```python
import torch

def _softmax(x, axis=-1):
    return torch.softmax(x, dim=axis)
def _gelu(x):
    return torch.nn.functional.gelu(x, approximate="tanh")
def _silu(x):
    return torch.nn.functional.silu(x)
def _sigmoid(x):
    return torch.sigmoid(x)
def _layernorm(x, axis=-1, eps=1e-5):
    m = x.mean(dim=axis, keepdim=True)
    v = x.var(dim=axis, keepdim=True, unbiased=False)
    return (x - m) / torch.sqrt(v + eps)
def _scatter_add(tgt, idx, src, axis=0):
    return tgt.clone().index_add_(axis, idx, src)
def _segment_sum(src, seg, n):
    out = torch.zeros((n,) + src.shape[1:], dtype=src.dtype)
    return out.index_add_(0, seg, src)
def _pad(x, pw):
    flat = tuple(v for pair in reversed(pw) for v in pair)
    return torch.nn.functional.pad(x, flat)

kernel_name = 'interleave_layers__ssm_conv_h3__sliding_block__prompt'

def setup_inputs():

    _w = (lambda a: (lambda t: t.to(torch.bfloat16) if t.is_floating_point() else t)(torch.as_tensor(a)))
    def _A_setup():
        B, S, D = 1, 2048, 448
        ks = 4
        return {
            "q": _w((lambda *s: torch.randn(*s, dtype=torch.bfloat16) * 0.05)(B, S, D)),
            "k": _w((lambda *s: torch.randn(*s, dtype=torch.bfloat16) * 0.4)(B, S, D)),
            "v": _w((lambda *s: torch.randn(*s, dtype=torch.bfloat16) * 0.4)(B, S, D)),
            "shift_k": _w((lambda *s: torch.randn(*s, dtype=torch.bfloat16) * 0.4)(D, ks) * 0.5),
            "ssm_k": _w((lambda *s: torch.randn(*s, dtype=torch.bfloat16) * 0.4)(D, S) * (1.0 / S ** 0.5)),
            "ssm_D": _w((lambda *s: torch.randn(*s, dtype=torch.bfloat16) * 0.4)(D)),
        }
    _A_in = _A_setup()

    def _B_setup():
        B, S, D = 1, 2048, 448
        return {
            "x": _w((lambda *s: torch.randn(*s, dtype=torch.bfloat16) * 0.05)(B, S, D)),
            "wqkv": _w((lambda *s: torch.randn(*s, dtype=torch.bfloat16) * 0.05)(D, 3 * D)),
            "wo": _w((lambda *s: torch.randn(*s, dtype=torch.bfloat16) * 0.05)(D, D)),
        }
    _B_in = _B_setup()

    return {
        "A_q": _A_in["q"],
        "A_k": _A_in["k"],
        "A_v": _A_in["v"],
        "A_shift_k": _A_in["shift_k"],
        "A_ssm_k": _A_in["ssm_k"],
        "A_ssm_D": _A_in["ssm_D"],
        "B_x": _B_in["x"],
        "B_wqkv": _B_in["wqkv"],
        "B_wo": _B_in["wo"],
    }

def reference(A_q, A_k, A_v, A_shift_k, A_ssm_k, A_ssm_D, B_x, B_wqkv, B_wo):
    def _A_ref(q, k, v, shift_k, ssm_k, ssm_D):
        B, S, D = q.shape
        ks = shift_k.shape[-1]
        k_pad = _pad(k, ((0, 0), (ks - 1, 0), (0, 0)))
        k_shift = torch.zeros_like(k)
        for t in range(ks):
            k_shift = k_shift + k_pad[:, t:t + S, :] * shift_k[:, ks - 1 - t][None, None, :]
        kv = k_shift * v
        L = 2 * S
        kv_pad = _pad(kv, ((0, 0), (0, S), (0, 0)))
        kf_pad = _pad(ssm_k, ((0, 0), (0, S)))
        kv_f = (lambda x, axes=(-2,-1): torch.fft.rfft2(x.float(), dim=axes))(kv_pad[:, None, :, :], axes=(1, 2))
        kf_f = (lambda x, axes=(-2,-1): torch.fft.rfft2(x.float(), dim=axes))(kf_pad[:, None, :], axes=(1, 2))
        kf_f = (lambda x, *d: x.permute(*d))(kf_f, 1, 2, 0)
        y_f = kv_f * kf_f[None, :, :, :]
        y = (lambda x, s, axes=(-2,-1): torch.fft.irfft2(x, s=s, dim=axes).to(torch.bfloat16))(y_f, (1, L), axes=(1, 2))[:, 0, :S, :]
        y = y + kv * ssm_D[None, None, :]
        return q * y

    def _B_ref(x, wqkv, wo):
        B, S, D = x.shape
        H = 16; Dh = D // H; W = 256
        qkv = (lambda x, *d: x.permute(*d))((x @ wqkv).reshape(B, S, 3, H, Dh), 2, 0, 3, 1, 4)
        q, k, v = qkv[0], qkv[1], qkv[2]
        scores = torch.einsum("bhqd,bhkd->bhqk", q, k) * (Dh ** -0.5)
        idx = torch.arange(S)
        mask = (idx[:, None] - idx[None, :] >= 0) & (idx[:, None] - idx[None, :] < W)
        scores = torch.where(mask[None, None], scores, -1e9)
        p = _softmax(scores, axis=-1)
        o = torch.einsum("bhqk,bhkd->bhqd", p, v)
        return (lambda x, *d: x.permute(*d))(o, 0, 2, 1, 3).reshape(B, S, D) @ wo

    x = A_q
    x = x + _A_ref(_layernorm(x, axis=-1), A_k, A_v, A_shift_k, A_ssm_k, A_ssm_D)
    x = x + _B_ref(_layernorm(x, axis=-1), B_wqkv, B_wo)
    x = x + _A_ref(_layernorm(x, axis=-1), A_k, A_v, A_shift_k, A_ssm_k, A_ssm_D)
    x = x + _B_ref(_layernorm(x, axis=-1), B_wqkv, B_wo)
    return x
```

```cpp
#include <hip/hip_runtime.h>
#include <hip/hip_bf16.h>
#include <stdint.h>

typedef unsigned short u16;
constexpr int S = 2048, D = 448, H = 16, DH = 28, W = 256;

__device__ __forceinline__ float bf2f(u16 b) { return __uint_as_float(((unsigned)b) << 16); }
__device__ __forceinline__ u16 f2bf(float f) {
  unsigned u = __float_as_uint(f);
  u += 0x7FFFu + ((u >> 16) & 1u);
  return (u16)(u >> 16);
}
__device__ __forceinline__ float rbf(float f) { return bf2f(f2bf(f)); }

__global__ void k_kv(const u16* k, const u16* v, const u16* sk, float* kvT) {
  int idx = blockIdx.x * blockDim.x + threadIdx.x;
  if (idx >= S * D) return;
  int t = idx / D, d = idx % D;
  float acc = 0.f;
  for (int j = 0; j < 4; ++j) {
    int s = t + j - 3;
    float kk = s >= 0 ? bf2f(k[s * D + d]) : 0.f;
    acc = rbf(acc + rbf(kk * bf2f(sk[d * 4 + 3 - j])));
  }
  kvT[d * S + t] = rbf(acc * bf2f(v[idx]));
}
__global__ void k_conv(const float* kvT, const u16* ssmk, const u16* ssmD, float* y) {
  __shared__ float u[S], h[S];
  int d = blockIdx.x;
  for (int i = threadIdx.x; i < S; i += blockDim.x) { u[i] = kvT[d * S + i]; h[i] = bf2f(ssmk[d * S + i]); }
  __syncthreads();
  float Dd = bf2f(ssmD[d]);
  for (int t = threadIdx.x; t < S; t += blockDim.x) {
    float acc = 0.f;
    for (int s = 0; s <= t; ++s) acc += h[t - s] * u[s];
    y[t * D + d] = rbf(rbf(acc) + rbf(u[t] * Dd));
  }
}
__device__ float block_sum(float v, float* sh) {
  for (int o = 32; o > 0; o >>= 1) v += __shfl_xor(v, o);
  __syncthreads();
  if ((threadIdx.x & 63) == 0) sh[threadIdx.x >> 6] = v;
  __syncthreads();
  float s = 0;
  for (int i = 0; i < (int)(blockDim.x >> 6); ++i) s += sh[i];
  return s;
}
__global__ void k_gate(const float* xin, const u16* xin_bf, const float* y, float* xout, float* hn) {
  __shared__ float sh[16];
  int t = blockIdx.x, d = threadIdx.x;
  float x = xin_bf ? bf2f(xin_bf[t * D + d]) : xin[t * D + d];
  float mf = block_sum(x, sh) / D;
  float vf = block_sum((x - mf) * (x - mf), sh) / D;
  float m = rbf(mf), v = rbf(vf);
  float sd = rbf(sqrtf(rbf(v + 1e-5f)));
  float n = rbf(rbf(x - m) / sd);
  float x1 = rbf(x + rbf(n * y[t * D + d]));
  xout[t * D + d] = x1;
  mf = block_sum(x1, sh) / D;
  vf = block_sum((x1 - mf) * (x1 - mf), sh) / D;
  m = rbf(mf); v = rbf(vf);
  sd = rbf(sqrtf(rbf(v + 1e-5f)));
  hn[t * D + d] = rbf(rbf(x1 - m) / sd);
}
__global__ void k_gemm(const float* A, const u16* B, float* C, int N, const float* res, u16* outbf) {
  __shared__ float a[D];
  int t = blockIdx.x;
  for (int i = threadIdx.x; i < D; i += blockDim.x) a[i] = A[t * D + i];
  __syncthreads();
  for (int n = threadIdx.x; n < N; n += blockDim.x) {
    float acc = 0.f;
    for (int k = 0; k < D; ++k) acc += a[k] * bf2f(B[k * N + n]);
    acc = rbf(acc);
    if (res) acc = rbf(acc + res[t * N + n]);
    if (outbf) outbf[t * N + n] = f2bf(acc); else C[t * N + n] = acc;
  }
}
__global__ void k_attn(const float* qkv, float* o) {
  __shared__ float sh[16];
  __shared__ float p[W];
  int t = blockIdx.x, h = blockIdx.y, j = threadIdx.x;
  int key = t - j;
  float s = -1e30f;
  if (key >= 0) {
    float acc = 0.f;
    for (int d = 0; d < DH; ++d) acc += qkv[t * 3 * D + h * DH + d] * qkv[key * 3 * D + D + h * DH + d];
    s = rbf(rbf(acc) * rbf(0.18898223650461361f));
  }
  float m = s;
  for (int o2 = 32; o2 > 0; o2 >>= 1) m = fmaxf(m, __shfl_xor(m, o2));
  if ((j & 63) == 0) sh[j >> 6] = m;
  __syncthreads();
  m = fmaxf(fmaxf(sh[0], sh[1]), fmaxf(sh[2], sh[3]));
  float e = key >= 0 ? __expf(s - m) : 0.f;
  float sum = block_sum(e, sh);
  p[j] = rbf(e / sum);
  __syncthreads();
  if (j < DH) {
    float acc = 0.f;
    for (int i = 0; i < W; ++i) { int kk = t - i; if (kk >= 0) acc += p[i] * qkv[kk * 3 * D + 2 * D + h * DH + j]; }
    o[t * D + h * DH + j] = rbf(acc);
  }
}

extern "C" void kernel_launch(void* const* d_in, const int* in_sizes, int n_in, void* d_out, int out_size, void* d_ws,
                              size_t ws_size, hipStream_t stream) {
  const u16* q = (const u16*)d_in[0];
  const u16* k = (const u16*)d_in[1];
  const u16* v = (const u16*)d_in[2];
  const u16* sk = (const u16*)d_in[3];
  const u16* ssmk = (const u16*)d_in[4];
  const u16* ssmD = (const u16*)d_in[5];
  const u16* wqkv = (const u16*)d_in[7];
  const u16* wo = (const u16*)d_in[8];
  float* ws = (float*)d_ws;
  const size_t SD = (size_t)S * D;
  float* kvT = ws; float* y = ws + SD; float* x1 = ws + 2 * SD; float* hn = ws + 3 * SD; float* o = ws + 4 * SD;
  float* x2 = ws + 5 * SD; float* x3 = ws + 6 * SD; float* qkv = ws + 7 * SD;
  k_kv<<<(S * D + 255) / 256, 256, 0, stream>>>(k, v, sk, kvT);
  k_conv<<<D, 256, 0, stream>>>(kvT, ssmk, ssmD, y);
  k_gate<<<S, D, 0, stream>>>(nullptr, q, y, x1, hn);
  k_gemm<<<S, 256, 0, stream>>>(hn, wqkv, qkv, 3 * D, nullptr, nullptr);
  k_attn<<<dim3(S, H), 256, 0, stream>>>(qkv, o);
  k_gemm<<<S, 256, 0, stream>>>(o, wo, x2, D, x1, nullptr);
  k_gate<<<S, D, 0, stream>>>(x2, nullptr, y, x3, hn);
  k_gemm<<<S, 256, 0, stream>>>(hn, wqkv, qkv, 3 * D, nullptr, nullptr);
  k_attn<<<dim3(S, H), 256, 0, stream>>>(qkv, o);
  k_gemm<<<S, 256, 0, stream>>>(o, wo, nullptr, D, x3, (u16*)d_out);
}
```

```cpp
#include <hip/hip_runtime.h>
#include <stdint.h>

typedef unsigned short u16;
typedef unsigned v4u __attribute__((ext_vector_type(4)));
typedef unsigned v2u __attribute__((ext_vector_type(2)));
typedef float f32x4 __attribute__((ext_vector_type(4)));
typedef float f32x2 __attribute__((ext_vector_type(2)));
typedef short bf16x8 __attribute__((ext_vector_type(8)));
typedef __bf16 bf16x2_t __attribute__((ext_vector_type(2)));
#define GAS __attribute__((address_space(1)))
#define LAS __attribute__((address_space(3)))
typedef GAS unsigned gu32;
#define RLX_AGENT __ATOMIC_RELAXED, __HIP_MEMORY_SCOPE_AGENT

constexpr int S = 2048, D = 448, NH = 16, DH = 28;
constexpr size_t SD2 = (size_t)S * D * 2;
constexpr size_t OFF_CTRL = 0;
constexpr size_t OFF_KVR = 65536;
constexpr size_t OFF_X0T = OFF_KVR + SD2;
constexpr size_t OFF_ST0 = OFF_X0T + SD2;
constexpr size_t OFF_WQF = OFF_ST0 + 16384;
constexpr size_t OFF_WOF = OFF_WQF + 16 * 84 * 1024;
constexpr size_t OFF_CS = OFF_WOF + 28 * 16 * 1024;
constexpr size_t OFF_X1F = OFF_CS + 8192;
constexpr size_t OFF_YF = OFF_X1F + SD2;
constexpr size_t OFF_X2F = OFF_YF + SD2;
constexpr size_t OFF_ST2 = OFF_X2F + SD2;
constexpr size_t OFF_KV = OFF_ST2 + 65536;
constexpr size_t OFF_O = OFF_KV + 2 * (4u << 20);
constexpr size_t WS_USED = OFF_O + 2 * (2u << 20);

#define STAMP(ws, slot) do {} while (0)

__device__ __forceinline__ float bflo(unsigned u) { return __uint_as_float(u << 16); }
__device__ __forceinline__ float bfhi(unsigned u) { return __uint_as_float(u & 0xffff0000u); }
__device__ __forceinline__ unsigned pk2(float a, float b) {
  f32x2 v = {a, b};
  bf16x2_t r = __builtin_convertvector(v, bf16x2_t);
  return __builtin_bit_cast(unsigned, r);
}
__device__ __forceinline__ f32x4 mfma(v4u a, v4u b, f32x4 c) {
  return __builtin_amdgcn_mfma_f32_16x16x32_bf16(__builtin_bit_cast(bf16x8, a), __builtin_bit_cast(bf16x8, b), c, 0, 0, 0);
}
__device__ __forceinline__ float wave_sum(float v) {
#pragma unroll
  for (int o = 32; o > 0; o >>= 1) v += __shfl_xor(v, o);
  return v;
}

__global__ void __launch_bounds__(512) k_prep(const u16* __restrict__ x0, const u16* __restrict__ kin,
                                              const u16* __restrict__ vin, const u16* __restrict__ shk,
                                              const u16* __restrict__ wqkv, const u16* __restrict__ wo,
                                              unsigned char* __restrict__ ws) {
  __shared__ __attribute__((aligned(16))) unsigned char sm[2 * 56 * 132];
  STAMP(ws, 0);
  const int b = blockIdx.x, tid = threadIdx.x, lane = tid & 63, w = tid >> 6;
  const int tb = b >> 3, cg = b & 7;
  const int r = tid / 7, c = tid - r * 7;
  const bool act = tid < 448;
  v4u kk[4], vv, xx, sw[4];
  const int t = tb * 64 + r;
  const int ch0 = cg * 56 + c * 8;
  if (act) {
#pragma unroll
    for (int j = 0; j < 4; ++j) {
      const int ts = t - j;
      kk[j] = ts >= 0 ? *(const v4u*)(kin + (size_t)ts * D + ch0) : v4u{0u, 0u, 0u, 0u};
    }
    vv = *(const v4u*)(vin + (size_t)t * D + ch0);
    xx = *(const v4u*)(x0 + (size_t)t * D + ch0);
#pragma unroll
    for (int j = 0; j < 4; ++j) sw[j] = *(const v4u*)(shk + ch0 * 4 + j * 8);
  }
  const int srow = b * 8 + w;
  v4u sx = v4u{0u, 0u, 0u, 0u};
  if (lane < 56) sx = *(const v4u*)(x0 + (size_t)srow * D + lane * 8);
  u16 wf[8];
  const int f = b * 7 + w;
  const int fr = lane & 15, fg = lane >> 4;
  if (w < 7) {
    if (f < 1344) {
      const int ks = f % 14, ht = f / 14, tile = ht % 6, h = ht / 6;
      const int dh = (tile & 1) * 16 + fr;
      const int n = (tile >> 1) * D + h * DH + dh;
#pragma unroll
      for (int j = 0; j < 8; ++j) {
        const int k = ks * 32 + fg * 8 + j;
        wf[j] = dh < DH ? wqkv[(size_t)k * (3 * D) + n] : (u16)((tile == 3 && dh == 28) ? 0x3F80 : 0);
      }
    } else {
      const int f2 = f - 1344, hh = f2 & 15, ct = f2 >> 4;
      const int n = ct * 16 + fr;
#pragma unroll
      for (int j = 0; j < 8; ++j) {
        const int dh = (j < 4) ? fg * 4 + j : 16 + fg * 4 + (j - 4);
        wf[j] = dh < DH ? wo[(size_t)(hh * DH + dh) * D + n] : (u16)0;
      }
    }
  }
  if (b < 32) ((unsigned*)(ws + OFF_CTRL))[b * 512 + tid] = 0u;
  if (act) {
    u16* skv = (u16*)sm;
    u16* sx0 = (u16*)(sm + 56 * 132);
#pragma unroll
    for (int e = 0; e < 8; e += 2) {
      const v4u s4 = sw[e >> 1];
      const int dw = e >> 1;
      float a0 = bflo(kk[3][dw]) * bfhi(s4.y);
      a0 = fmaf(bflo(kk[2][dw]), bflo(s4.y), a0);
      a0 = fmaf(bflo(kk[1][dw]), bfhi(s4.x), a0);
      a0 = fmaf(bflo(kk[0][dw]), bflo(s4.x), a0);
      float a1 = bfhi(kk[3][dw]) * bfhi(s4.w);
      a1 = fmaf(bfhi(kk[2][dw]), bflo(s4.w), a1);
      a1 = fmaf(bfhi(kk[1][dw]), bfhi(s4.z), a1);
      a1 = fmaf(bfhi(kk[0][dw]), bflo(s4.z), a1);
      const unsigned p = pk2(a0 * bflo(vv[dw]), a1 * bfhi(vv[dw]));
      skv[(c * 8 + e) * 66 + (63 - r)] = (u16)p;
      skv[(c * 8 + e + 1) * 66 + (63 - r)] = (u16)(p >> 16);
      sx0[(c * 8 + e) * 66 + r] = (u16)xx[dw];
      sx0[(c * 8 + e + 1) * 66 + r] = (u16)(xx[dw] >> 16);
    }
  }
  {
    float s = 0.f;
#pragma unroll
    for (int j = 0; j < 4; ++j) s += bflo(sx[j]) + bfhi(sx[j]);
    const float mean = wave_sum(s) * (1.0f / D);
    float q = 0.f;
    if (lane < 56) {
#pragma unroll
      for (int j = 0; j < 4; ++j) {
        const float d0 = bflo(sx[j]) - mean, d1 = bfhi(sx[j]) - mean;
        q = fmaf(d0, d0, q);
        q = fmaf(d1, d1, q);
      }
    }
    const float var = wave_sum(q) * (1.0f / D);
    if (lane == 0) ((f32x2*)(ws + OFF_ST0))[srow] = f32x2{mean, rsqrtf(var + 1e-5f)};
  }
  if (w < 7) {
    v4u o;
#pragma unroll
    for (int j = 0; j < 4; ++j) o[j] = (unsigned)wf[2 * j] | ((unsigned)wf[2 * j + 1] << 16);
    *(v4u*)(ws + OFF_WQF + (size_t)f * 1024 + lane * 16) = o;
  }
  __syncthreads();
  if (act) {
    const int ch = tid >> 3, ck = tid & 7;
    const unsigned* p0 = (const unsigned*)(sm + ch * 132 + ck * 16);
    const unsigned* p1 = (const unsigned*)(sm + 56 * 132 + ch * 132 + ck * 16);
    const v4u a = {p0[0], p0[1], p0[2], p0[3]};
    const v4u x = {p1[0], p1[1], p1[2], p1[3]};
    const size_t row = (size_t)(cg * 56 + ch) * S;
    *(v4u*)(ws + OFF_KVR + (row + (S - 64 - tb * 64) + ck * 8) * 2) = a;
    *(v4u*)(ws + OFF_X0T + (row + tb * 64 + ck * 8) * 2) = x;
  }
  STAMP(ws, 1);
}

constexpr int CV_CS = 4160;
constexpr int CV_UR = 2304 * 2;
constexpr int CV_OFF_U = 8 * CV_CS;
constexpr int CV_OFF_T = CV_OFF_U + 2 * CV_UR;
constexpr int CV_LDS = CV_OFF_T + 4 * 4096;

__global__ void __launch_bounds__(256) k_conv(const u16* __restrict__ ssmk, const u16* __restrict__ ssmD,
                                              unsigned char* __restrict__ ws) {
  __shared__ __attribute__((aligned(16))) unsigned char sm[CV_LDS];
  STAMP(ws, 2);
  const int b = blockIdx.x, tid = threadIdx.x, lane = tid & 63;
  const int w = __builtin_amdgcn_readfirstlane(tid >> 6);
  if (b >= 224) {
    if (w < 3) {
      const int tile = (b - 224) * 3 + w;
      float s = 0.f;
      v4u f[14];
#pragma unroll
      for (int ks = 0; ks < 14; ++ks) f[ks] = *(const v4u*)(ws + OFF_WQF + ((size_t)tile * 14 + ks) * 1024 + lane * 16);
#pragma unroll
      for (int ks = 0; ks < 14; ++ks)
#pragma unroll
        for (int j = 0; j < 4; ++j) s += bflo(f[ks][j]) + bfhi(f[ks][j]);
      s += __shfl_xor(s, 16);
      s += __shfl_xor(s, 32);
      if (lane < 16) ((float*)(ws + OFF_CS))[tile * 16 + lane] = s;
    }
    STAMP(ws, 3); STAMP(ws, 4); STAMP(ws, 5);
    return;
  }
  const int cp = (b & 7) * 28 + (b >> 3);
  const int c = lane & 15, g = lane >> 4;
  v4u hd[2], ud[2];
  v2u hn[2];
#pragma unroll
  for (int ch = 0; ch < 2; ++ch) {
    const u16* hp = ssmk + (size_t)(2 * cp + ch) * S + tid * 8;
    hd[ch] = *(const v4u*)hp;
    hn[ch] = tid < 255 ? *(const v2u*)(hp + 8) : v2u{0u, 0u};
    ud[ch] = *(const v4u*)(ws + OFF_KVR + ((size_t)(2 * cp + ch) * S + tid * 8) * 2);
  }
  v2u x0v[2][2];
  v4u stv[2][2];
#pragma unroll
  for (int qq = 0; qq < 2; ++qq) {
    const int t = 256 * (2 * w + qq) + 16 * c + 4 * g;
#pragma unroll
    for (int ch = 0; ch < 2; ++ch) x0v[qq][ch] = *(const v2u*)(ws + OFF_X0T + ((size_t)(2 * cp + ch) * S + t) * 2);
    stv[qq][0] = *(const v4u*)(ws + OFF_ST0 + (size_t)t * 8);
    stv[qq][1] = *(const v4u*)(ws + OFF_ST0 + (size_t)t * 8 + 16);
  }
  const unsigned dD = *(const unsigned*)(ssmD + 2 * cp);
#pragma unroll
  for (int ch = 0; ch < 2; ++ch) {
    const unsigned d0 = hd[ch].x, d1 = hd[ch].y, d2 = hd[ch].z, d3 = hd[ch].w, d4 = hn[ch].x, d5 = hn[ch].y;
    const unsigned a01 = __builtin_amdgcn_alignbit(d1, d0, 16), a12 = __builtin_amdgcn_alignbit(d2, d1, 16);
    const unsigned a23 = __builtin_amdgcn_alignbit(d3, d2, 16), a34 = __builtin_amdgcn_alignbit(d4, d3, 16);
    const unsigned a45 = __builtin_amdgcn_alignbit(d5, d4, 16);
    unsigned char* base = sm + ch * 4 * CV_CS + 32 + tid * 16;
    *(v4u*)(base + 0 * CV_CS) = v4u{a01, a12, a23, a34};
    *(v4u*)(base + 1 * CV_CS) = v4u{d1, d2, d3, d4};
    *(v4u*)(base + 2 * CV_CS) = v4u{a12, a23, a34, a45};
    *(v4u*)(base + 3 * CV_CS) = v4u{d2, d3, d4, d5};
    if (tid == 0) {
      unsigned char* bz = sm + ch * 4 * CV_CS;
      const v4u z = {0u, 0u, 0u, 0u};
      *(v4u*)(bz + 0 * CV_CS) = z;
      *(v4u*)(bz + 1 * CV_CS) = z;
      *(v4u*)(bz + 2 * CV_CS) = z;
      *(v4u*)(bz + 3 * CV_CS) = z;
      *(v4u*)(bz + 0 * CV_CS + 16) = v4u{0u, 0u, 0u, d0 << 16};
      *(v4u*)(bz + 1 * CV_CS + 16) = v4u{0u, 0u, 0u, d0};
      *(v4u*)(bz + 2 * CV_CS + 16) = v4u{0u, 0u, d0 << 16, a01};
      *(v4u*)(bz + 3 * CV_CS + 16) = v4u{0u, 0u, d0, d1};
    }
    *(v4u*)(sm + CV_OFF_U + ch * CV_UR + tid * 16) = ud[ch];
    if (tid < 32) *(v4u*)(sm + CV_OFF_U + ch * CV_UR + 4096 + tid * 16) = v4u{0u, 0u, 0u, 0u};
  }
  __syncthreads();
  STAMP(ws, 3);
  const int mych = w >> 1, kh = w & 1;
  const unsigned char* abase = sm + mych * 4 * CV_CS + (c & 3) * CV_CS + ((c & 12) + 8 * g) * 2 + kh * 256;
  const unsigned char* bbase = sm + CV_OFF_U + mych * CV_UR + (2032 - 16 * c + 8 * g) * 2 + kh * 256;
  f32x4 acc[8];
#pragma unroll
  for (int q = 0; q < 8; ++q) acc[q] = f32x4{0.f, 0.f, 0.f, 0.f};
#pragma unroll
  for (int kp = 0; kp < 4; ++kp) {
    v4u A[8], B[8];
#pragma unroll
    for (int p = 0; p < 8; ++p) {
      const v2u lo = *(const v2u*)(abase + p * 512 + kp * 64);
      const v2u hi = *(const v2u*)(abase + p * 512 + kp * 64 + 8);
      A[p] = v4u{lo.x, lo.y, hi.x, hi.y};
      B[p] = *(const v4u*)(bbase - p * 512 + kp * 64);
    }
#pragma unroll
    for (int p = 0; p < 8; ++p)
#pragma unroll
      for (int q = p; q < 8; ++q) acc[q] = mfma(A[p], B[q - p], acc[q]);
  }
  __syncthreads();
  STAMP(ws, 4);
  f32x4* red = (f32x4*)sm;
#pragma unroll
  for (int q = 0; q < 8; ++q) red[(w * 8 + q) * 64 + lane] = acc[q];
  __syncthreads();
  unsigned* tsc = (unsigned*)(sm + CV_OFF_T + w * 4096);
  const float D0 = bflo(dD), D1 = bfhi(dD);
#pragma unroll
  for (int qq = 0; qq < 2; ++qq) {
    const int q = 2 * w + qq;
    const int t = 256 * q + 16 * c + 4 * g;
    float yv[2][4], xv[2][4];
#pragma unroll
    for (int ch = 0; ch < 2; ++ch) {
      const f32x4 s = red[((2 * ch) * 8 + q) * 64 + lane] + red[((2 * ch + 1) * 8 + q) * 64 + lane];
      const v2u ur = *(const v2u*)(sm + CV_OFF_U + ch * CV_UR + (2044 - t) * 2);
      const float u0 = bfhi(ur.y), u1 = bflo(ur.y), u2 = bfhi(ur.x), u3 = bflo(ur.x);
      const float Dd = ch ? D1 : D0;
      yv[ch][0] = fmaf(u0, Dd, s[0]);
      yv[ch][1] = fmaf(u1, Dd, s[1]);
      yv[ch][2] = fmaf(u2, Dd, s[2]);
      yv[ch][3] = fmaf(u3, Dd, s[3]);
      xv[ch][0] = bflo(x0v[qq][ch].x);
      xv[ch][1] = bfhi(x0v[qq][ch].x);
      xv[ch][2] = bflo(x0v[qq][ch].y);
      xv[ch][3] = bfhi(x0v[qq][ch].y);
    }
    v4u yo, xo;
#pragma unroll
    for (int j = 0; j < 4; ++j) {
      const float mean = __uint_as_float(stv[qq][j >> 1][(j & 1) * 2]);
      const float rstd = __uint_as_float(stv[qq][j >> 1][(j & 1) * 2 + 1]);
      const unsigned yp = pk2(yv[0][j], yv[1][j]);
      yo[j] = yp;
      const float nb = -mean * rstd;
      const float r0 = fmaf(fmaf(xv[0][j], rstd, nb), bflo(yp), xv[0][j]);
      const float r1 = fmaf(fmaf(xv[1][j], rstd, nb), bfhi(yp), xv[1][j]);
      xo[j] = pk2(r0, r1);
    }
    const int tl = 256 * qq + 16 * c + 4 * g;
    *(v4u*)(tsc + tl) = yo;
    *(v4u*)(tsc + 512 + tl) = xo;
  }
  const size_t obase = ((size_t)(cp >> 2) * S + 512 * w + lane) * 16 + (cp & 3) * 4;
#pragma unroll
  for (int i = 0; i < 8; ++i) {
    *(unsigned*)(ws + OFF_YF + obase + i * 1024) = tsc[i * 64 + lane];
    *(unsigned*)(ws + OFF_X1F + obase + i * 1024) = tsc[512 + i * 64 + lane];
  }
  STAMP(ws, 5);
}

constexpr int QA_OFF_K = 84 * 1024;
constexpr int QA_OFF_V = QA_OFF_K + 24 * 1024;
constexpr int QA_LDS = QA_OFF_V + 24 * 1024;

__device__ __forceinline__ void combine_stats(const v4u s0, const v4u s1, float& a, float& bb) {
  const float m0 = __uint_as_float(s0.x), m1 = __uint_as_float(s0.z), m2 = __uint_as_float(s1.x), m3 = __uint_as_float(s1.z);
  const float mean = 0.25f * ((m0 + m1) + (m2 + m3));
  const float d0 = m0 - mean, d1 = m1 - mean, d2 = m2 - mean, d3 = m3 - mean;
  const float M2 = ((__uint_as_float(s0.y) + __uint_as_float(s0.w)) + (__uint_as_float(s1.y) + __uint_as_float(s1.w))) +
                   112.f * ((d0 * d0 + d1 * d1) + (d2 * d2 + d3 * d3));
  a = rsqrtf(M2 * (1.0f / D) + 1e-5f);
  bb = -mean * a;
}
__device__ __forceinline__ unsigned gate2(unsigned x, unsigned y, float a, float bb) {
  const float x0 = bflo(x), x1 = bfhi(x);
  return pk2(fmaf(fmaf(x0, a, bb), bflo(y), x0), fmaf(fmaf(x1, a, bb), bfhi(y), x1));
}

template <int LAYER>
__global__ void __launch_bounds__(512, 2) k_qa(unsigned char* __restrict__ ws) {
  extern __shared__ __attribute__((aligned(16))) unsigned char sm[];
  STAMP(ws, 6 + LAYER * 9);
  const int b = blockIdx.x, tid = threadIdx.x, lane = tid & 63;
  const int w = __builtin_amdgcn_readfirstlane(tid >> 6);
  const int ti = (b & 7) * 2 + (b >> 7), h = (b >> 3) & 15;
  const int c = lane & 15, g = lane >> 4;
  const int t0 = ti * 128 + w * 16;
  const __amdgpu_buffer_rsrc_t rs = __builtin_amdgcn_make_buffer_rsrc((void*)ws, 0, (int)WS_USED, 0x00020000);
  v4u X[14];
  {
    const unsigned char* xp = ws + (LAYER == 0 ? OFF_X1F : OFF_X2F) + ((size_t)g * S + t0 + c) * 16;
#pragma unroll
    for (int ks = 0; ks < 14; ++ks) X[ks] = *(const v4u*)(xp + (size_t)ks * 4 * S * 16);
  }
  {
    const unsigned char* wp = ws + OFF_WQF + (size_t)h * 84 * 1024 + tid * 16;
    v4u tmp[11];
#pragma unroll
    for (int i = 0; i < 11; ++i)
      if (i < 10 || tid < 256) tmp[i] = *(const v4u*)(wp + i * 8192);
#pragma unroll
    for (int i = 0; i < 11; ++i)
      if (i < 10 || tid < 256) *(v4u*)(sm + i * 8192 + tid * 16) = tmp[i];
  }
  if (ti < 2) {
#pragma unroll
    for (int i = 0; i < 2; ++i) {
      *(v4u*)(sm + QA_OFF_K + i * 8192 + tid * 16) = v4u{0u, 0u, 0u, 0u};
      *(v4u*)(sm + QA_OFF_V + i * 8192 + tid * 16) = v4u{0u, 0u, 0u, 0u};
    }
  }
  const float* csp = (const float*)(ws + OFF_CS) + h * 96;
  f32x4 csq[2], csk[2];
  float csv[2];
#pragma unroll
  for (int tt = 0; tt < 2; ++tt) {
    csq[tt] = *(const f32x4*)(csp + tt * 16 + 4 * g);
    csk[tt] = *(const f32x4*)(csp + 32 + tt * 16 + 4 * g);
    csv[tt] = csp[64 + tt * 16 + c];
  }
  if (LAYER == 1) {
    const unsigned char* yp = ws + OFF_YF + ((size_t)g * S + t0 + c) * 16;
    const v4u s0 = *(const v4u*)(ws + OFF_ST2 + (size_t)(t0 + c) * 32);
    const v4u s1 = *(const v4u*)(ws + OFF_ST2 + (size_t)(t0 + c) * 32 + 16);
    v4u Y[14];
#pragma unroll
    for (int ks = 0; ks < 14; ++ks) Y[ks] = *(const v4u*)(yp + (size_t)ks * 4 * S * 16);
    float a, bb;
    combine_stats(s0, s1, a, bb);
#pragma unroll
    for (int ks = 0; ks < 14; ++ks)
#pragma unroll
      for (int j = 0; j < 4; ++j) X[ks][j] = gate2(X[ks][j], Y[ks][j], a, bb);
  }
  __syncthreads();
  STAMP(ws, 7 + LAYER * 9);
  const unsigned char* wl = sm + lane * 16;
  f32x4 ak[2], av[2], gram;
  ak[0] = ak[1] = av[0] = av[1] = gram = f32x4{0.f, 0.f, 0.f, 0.f};
#pragma unroll
  for (int ks = 0; ks < 14; ++ks) {
    const v4u wk0 = *(const v4u*)(wl + (2 * 14 + ks) * 1024);
    const v4u wk1 = *(const v4u*)(wl + (3 * 14 + ks) * 1024);
    const v4u wv0 = *(const v4u*)(wl + (4 * 14 + ks) * 1024);
    const v4u wv1 = *(const v4u*)(wl + (5 * 14 + ks) * 1024);
    gram = mfma(X[ks], X[ks], gram);
    ak[0] = mfma(wk0, X[ks], ak[0]);
    ak[1] = mfma(wk1, X[ks], ak[1]);
    av[0] = mfma(X[ks], wv0, av[0]);
    av[1] = mfma(X[ks], wv1, av[1]);
  }
  float mean, rstd;
  {
    const float sumx = __shfl(ak[1][0], 48 + c);
    const int rr = c & 3;
    const float gsel = rr == 0 ? gram[0] : (rr == 1 ? gram[1] : (rr == 2 ? gram[2] : gram[3]));
    const float sumsq = __shfl(gsel, (c >> 2) * 16 + c);
    mean = sumx * (1.0f / D);
    const float var = fmaxf(fmaf(-mean, mean, sumsq * (1.0f / D)), 0.f);
    rstd = rsqrtf(var + 1e-5f);
  }
  const int kvbase = (int)OFF_KV + ((LAYER * 16 + ti) * 16 + h) * 16384;
  {
    v4u kf;
    kf.x = pk2((ak[0][0] - mean * csk[0][0]) * rstd, (ak[0][1] - mean * csk[0][1]) * rstd);
    kf.y = pk2((ak[0][2] - mean * csk[0][2]) * rstd, (ak[0][3] - mean * csk[0][3]) * rstd);
    kf.z = pk2((ak[1][0] - mean * csk[1][0]) * rstd, (ak[1][1] - mean * csk[1][1]) * rstd);
    kf.w = pk2((ak[1][2] - mean * csk[1][2]) * rstd, (ak[1][3] - mean * csk[1][3]) * rstd);
    float v0[4], v1[4];
#pragma unroll
    for (int j = 0; j < 4; ++j) {
      const float mj = __shfl(mean, 4 * g + j), rj = __shfl(rstd, 4 * g + j);
      v0[j] = (av[0][j] - mj * csv[0]) * rj;
      v1[j] = c == 12 ? 1.0f : (av[1][j] - mj * csv[1]) * rj;
    }
    const v4u vf = {pk2(v0[0], v0[1]), pk2(v0[2], v0[3]), pk2(v1[0], v1[1]), pk2(v1[2], v1[3])};
    *(v4u*)(sm + QA_OFF_K + (16 + w) * 1024 + lane * 16) = kf;
    *(v4u*)(sm + QA_OFF_V + (16 + w) * 1024 + lane * 16) = vf;
    __builtin_amdgcn_raw_buffer_store_b128(kf, rs, kvbase + w * 1024 + lane * 16, 0, 16);
    __builtin_amdgcn_raw_buffer_store_b128(vf, rs, kvbase + 8192 + w * 1024 + lane * 16, 0, 16);
  }
  STAMP(ws, 8 + LAYER * 9);
  v4u qf;
  {
    f32x4 aq[2];
    aq[0] = aq[1] = f32x4{0.f, 0.f, 0.f, 0.f};
#pragma unroll
    for (int ks = 0; ks < 14; ++ks) {
      const v4u wq0 = *(const v4u*)(wl + (0 * 14 + ks) * 1024);
      const v4u wq1 = *(const v4u*)(wl + (1 * 14 + ks) * 1024);
      aq[0] = mfma(wq0, X[ks], aq[0]);
      aq[1] = mfma(wq1, X[ks], aq[1]);
    }
    const float qs = rstd * (0.18898223650461361f * 1.4426950408889634f);
    qf.x = pk2((aq[0][0] - mean * csq[0][0]) * qs, (aq[0][1] - mean * csq[0][1]) * qs);
    qf.y = pk2((aq[0][2] - mean * csq[0][2]) * qs, (aq[0][3] - mean * csq[0][3]) * qs);
    qf.z = pk2((aq[1][0] - mean * csq[1][0]) * qs, (aq[1][1] - mean * csq[1][1]) * qs);
    qf.w = pk2((aq[1][2] - mean * csq[1][2]) * qs, (aq[1][3] - mean * csq[1][3]) * qs);
  }
  gu32* flags = (gu32*)(ws + OFF_CTRL) + LAYER * 256 * 32;
  asm volatile("s_waitcnt vmcnt(0)" ::: "memory");
  __syncthreads();
  STAMP(ws, 9 + LAYER * 9);
  if (tid == 0) __hip_atomic_store(flags + (ti * 16 + h) * 32, 1u, RLX_AGENT);
  if (ti > 0) {
    if (tid < 2 && ti - 1 - tid >= 0) {
      gu32* fp = flags + ((ti - 1 - tid) * 16 + h) * 32;
      unsigned spins = 0;
      while (__hip_atomic_load(fp, RLX_AGENT) == 0u) {
        if (++spins > (1u << 20)) break;
      }
    }
    __builtin_amdgcn_fence(__ATOMIC_ACQUIRE, "wavefront");
    __syncthreads();
    STAMP(ws, 10 + LAYER * 9);
    v4u nb[4];
    nb[0] = __builtin_amdgcn_raw_buffer_load_b128(rs, kvbase - 16 * 16384 + tid * 16, 0, 16);
    nb[1] = __builtin_amdgcn_raw_buffer_load_b128(rs, kvbase - 16 * 16384 + 8192 + tid * 16, 0, 16);
    if (ti > 1) {
      nb[2] = __builtin_amdgcn_raw_buffer_load_b128(rs, kvbase - 32 * 16384 + tid * 16, 0, 16);
      nb[3] = __builtin_amdgcn_raw_buffer_load_b128(rs, kvbase - 32 * 16384 + 8192 + tid * 16, 0, 16);
    }
    *(v4u*)(sm + QA_OFF_K + 8192 + tid * 16) = nb[0];
    *(v4u*)(sm + QA_OFF_V + 8192 + tid * 16) = nb[1];
    if (ti > 1) {
      *(v4u*)(sm + QA_OFF_K + tid * 16) = nb[2];
      *(v4u*)(sm + QA_OFF_V + tid * 16) = nb[3];
    }
    __syncthreads();
  } else {
    STAMP(ws, 10 + LAYER * 9);
  }
  STAMP(ws, 11 + LAYER * 9);
  const unsigned char* kl = sm + QA_OFF_K + w * 1024 + lane * 16;
  const unsigned char* vl = sm + QA_OFF_V + w * 1024 + lane * 16;
  f32x4 sc[17];
#pragma unroll
  for (int n = 0; n < 17; ++n) sc[n] = mfma(*(const v4u*)(kl + n * 1024), qf, f32x4{0.f, 0.f, 0.f, 0.f});
  const float NEG = -__builtin_inff();
#pragma unroll
  for (int j = 0; j < 4; ++j) {
    if (4 * g + j <= c) sc[0][j] = NEG;
    if (4 * g + j > c) sc[16][j] = NEG;
  }
  if (ti < 2) {
#pragma unroll
    for (int n = 0; n < 16; ++n)
      if (w + n < 16 - 8 * ti) sc[n] = f32x4{NEG, NEG, NEG, NEG};
  }
  float mx = NEG;
#pragma unroll
  for (int n = 0; n < 17; ++n) mx = fmaxf(fmaxf(mx, fmaxf(sc[n][0], sc[n][1])), fmaxf(sc[n][2], sc[n][3]));
  mx = fmaxf(mx, __shfl_xor(mx, 16));
  mx = fmaxf(mx, __shfl_xor(mx, 32));
  f32x4 o0 = {0.f, 0.f, 0.f, 0.f}, o1 = {0.f, 0.f, 0.f, 0.f};
#pragma unroll
  for (int n = 0; n < 17; n += 2) {
    v4u pf;
    pf.x = pk2(__builtin_amdgcn_exp2f(sc[n][0] - mx), __builtin_amdgcn_exp2f(sc[n][1] - mx));
    pf.y = pk2(__builtin_amdgcn_exp2f(sc[n][2] - mx), __builtin_amdgcn_exp2f(sc[n][3] - mx));
    const v4u va = *(const v4u*)(vl + n * 1024);
    v4u vb = va;
    if (n + 1 < 17) {
      pf.z = pk2(__builtin_amdgcn_exp2f(sc[n + 1][0] - mx), __builtin_amdgcn_exp2f(sc[n + 1][1] - mx));
      pf.w = pk2(__builtin_amdgcn_exp2f(sc[n + 1][2] - mx), __builtin_amdgcn_exp2f(sc[n + 1][3] - mx));
      vb = *(const v4u*)(vl + (n + 1) * 1024);
    } else {
      pf.z = 0u;
      pf.w = 0u;
    }
    o0 = mfma(v4u{va.x, va.y, vb.x, vb.y}, pf, o0);
    o1 = mfma(v4u{va.z, va.w, vb.z, vb.w}, pf, o1);
  }
  const float inv = 1.0f / __shfl(o1[0], 48 + c);
  const v4u of = {pk2(o0[0] * inv, o0[1] * inv), pk2(o0[2] * inv, o0[3] * inv), pk2(o1[0] * inv, o1[1] * inv),
                  pk2(o1[2] * inv, o1[3] * inv)};
  *(v4u*)(ws + OFF_O + (size_t)LAYER * (2u << 20) + ((size_t)(ti * 8 + w) * 16 + h) * 1024 + lane * 16) = of;
  STAMP(ws, 12 + LAYER * 9);
}

template <int LAYER>
__global__ void __launch_bounds__(512, 2) k_out(unsigned char* __restrict__ ws, u16* __restrict__ out) {
  __shared__ __attribute__((aligned(16))) unsigned char sm[32768 + 7 * 32 * 8];
  STAMP(ws, 13 + LAYER * 9);
  const int b = blockIdx.x, tid = threadIdx.x, lane = tid & 63;
  const int w = __builtin_amdgcn_readfirstlane(tid >> 6);
  const int ti = (b & 7) * 2 + (b >> 7), j = (b >> 3) & 15;
  const int rb = j >> 2, cq = j & 3;
  const int c = lane & 15, g = lane >> 4;
  const int t0 = ti * 128 + rb * 32;
  const int ct = cq * 7 + (w < 7 ? w : 6);
  v4u ot[4];
  {
    const unsigned char* op = ws + OFF_O + (size_t)LAYER * (2u << 20) + (size_t)(t0 >> 4) * 16384 + tid * 16;
#pragma unroll
    for (int i = 0; i < 4; ++i) ot[i] = *(const v4u*)(op + i * 8192);
  }
  v4u WF[16];
#pragma unroll
  for (int ks = 0; ks < 16; ++ks) WF[ks] = *(const v4u*)(ws + OFF_WOF + ((size_t)ct * 16 + ks) * 1024 + lane * 16);
  v2u xr[2], yr[2];
  v4u s0[2], s1[2];
#pragma unroll
  for (int tt = 0; tt < 2; ++tt) {
    const size_t xo = ((size_t)(ct * 2 + (g >> 1)) * S + t0 + tt * 16 + c) * 16 + (g & 1) * 8;
    xr[tt] = *(const v2u*)(ws + (LAYER == 0 ? OFF_X1F : OFF_X2F) + xo);
    if (LAYER == 1) {
      yr[tt] = *(const v2u*)(ws + OFF_YF + xo);
      s0[tt] = *(const v4u*)(ws + OFF_ST2 + (size_t)(t0 + tt * 16 + c) * 32);
      s1[tt] = *(const v4u*)(ws + OFF_ST2 + (size_t)(t0 + tt * 16 + c) * 32 + 16);
    }
  }
#pragma unroll
  for (int i = 0; i < 4; ++i) *(v4u*)(sm + i * 8192 + tid * 16) = ot[i];
  __syncthreads();
  f32x4 acc[2];
  acc[0] = acc[1] = f32x4{0.f, 0.f, 0.f, 0.f};
#pragma unroll
  for (int ks = 0; ks < 16; ++ks) {
    acc[0] = mfma(WF[ks], *(const v4u*)(sm + ks * 1024 + lane * 16), acc[0]);
    acc[1] = mfma(WF[ks], *(const v4u*)(sm + 16384 + ks * 1024 + lane * 16), acc[1]);
  }
  f32x2* sst = (f32x2*)(sm + 32768);
#pragma unroll
  for (int tt = 0; tt < 2; ++tt) {
    v2u xres = xr[tt];
    if (LAYER == 1) {
      float a, bb;
      combine_stats(s0[tt], s1[tt], a, bb);
      xres.x = gate2(xres.x, yr[tt].x, a, bb);
      xres.y = gate2(xres.y, yr[tt].y, a, bb);
    }
    v2u o;
    o.x = pk2(bflo(xres.x) + acc[tt][0], bfhi(xres.x) + acc[tt][1]);
    o.y = pk2(bflo(xres.y) + acc[tt][2], bfhi(xres.y) + acc[tt][3]);
    if (LAYER == 0) {
      if (w < 7) {
        const size_t xo = ((size_t)(ct * 2 + (g >> 1)) * S + t0 + tt * 16 + c) * 16 + (g & 1) * 8;
        *(v2u*)(ws + OFF_X2F + xo) = o;
      }
      const float e0 = bflo(o.x), e1 = bfhi(o.x), e2 = bflo(o.y), e3 = bfhi(o.y);
      float s = (e0 + e1) + (e2 + e3);
      s += __shfl_xor(s, 16);
      s += __shfl_xor(s, 32);
      const float m = s * (1.0f / 16.f);
      float q = (e0 - m) * (e0 - m) + (e1 - m) * (e1 - m) + (e2 - m) * (e2 - m) + (e3 - m) * (e3 - m);
      q += __shfl_xor(q, 16);
      q += __shfl_xor(q, 32);
      if (w < 7 && g == 0) sst[w * 32 + tt * 16 + c] = f32x2{m, q};
    } else {
      if (w < 7) *(v2u*)(out + (size_t)(t0 + tt * 16 + c) * D + ct * 16 + 4 * g) = o;
    }
  }
  if (LAYER == 0) {
    __syncthreads();
    if (tid < 32) {
      f32x2 p[7];
      float m = 0.f;
#pragma unroll
      for (int i = 0; i < 7; ++i) { p[i] = sst[i * 32 + tid]; m += p[i].x; }
      m *= (1.0f / 7.f);
      float q = 0.f;
#pragma unroll
      for (int i = 0; i < 7; ++i) q += p[i].y + 16.f * (p[i].x - m) * (p[i].x - m);
      ((f32x2*)(ws + OFF_ST2))[(t0 + tid) * 4 + cq] = f32x2{m, q};
    }
  }
  STAMP(ws, 14 + LAYER * 9);
}

extern "C" void kernel_launch(void* const* d_in, const int* in_sizes, int n_in, void* d_out, int out_size, void* d_ws,
                              size_t ws_size, hipStream_t stream) {
  unsigned char* ws = (unsigned char*)d_ws;
  (void)hipFuncSetAttribute((const void*)k_qa<0>, hipFuncAttributeMaxDynamicSharedMemorySize, QA_LDS);
  (void)hipFuncSetAttribute((const void*)k_qa<1>, hipFuncAttributeMaxDynamicSharedMemorySize, QA_LDS);
  k_prep<<<256, 512, 0, stream>>>((const u16*)d_in[0], (const u16*)d_in[1], (const u16*)d_in[2], (const u16*)d_in[3],
                                  (const u16*)d_in[7], (const u16*)d_in[8], ws);
  k_conv<<<256, 256, 0, stream>>>((const u16*)d_in[4], (const u16*)d_in[5], ws);
  k_qa<0><<<256, 512, QA_LDS, stream>>>(ws);
  k_out<0><<<256, 512, 0, stream>>>(ws, (u16*)d_out);
  k_qa<1><<<256, 512, QA_LDS, stream>>>(ws);
  k_out<1><<<256, 512, 0, stream>>>(ws, (u16*)d_out);
}
```

```cpp
#include <hip/hip_runtime.h>
#include <stdint.h>

typedef unsigned short u16;
typedef unsigned v4u __attribute__((ext_vector_type(4)));
typedef unsigned v2u __attribute__((ext_vector_type(2)));
typedef float f32x4 __attribute__((ext_vector_type(4)));
typedef float f32x2 __attribute__((ext_vector_type(2)));
typedef short bf16x8 __attribute__((ext_vector_type(8)));
typedef short v4i16 __attribute__((ext_vector_type(4)));
typedef __bf16 bf16x2_t __attribute__((ext_vector_type(2)));
#define LAS __attribute__((address_space(3)))
typedef LAS unsigned char* lptr;

constexpr int S = 2048, D = 448, NH = 16, DH = 28;
constexpr unsigned SD2 = (unsigned)S * D * 2;
constexpr unsigned OFF_KVR = 0;
constexpr unsigned OFF_X0P = OFF_KVR + SD2;
constexpr unsigned OFF_ST0 = OFF_X0P + SD2;
constexpr unsigned WQ_HEAD = 85 * 1024;
constexpr unsigned OFF_WQF = OFF_ST0 + 8192;
constexpr unsigned OFF_WOF = OFF_WQF + 16 * WQ_HEAD;
constexpr unsigned OFF_X1F = OFF_WOF + 28 * 16 * 1024;
constexpr unsigned OFF_YF = OFF_X1F + SD2;
constexpr unsigned OFF_X2F = OFF_YF + SD2;
constexpr unsigned OFF_ST2 = OFF_X2F + SD2;
constexpr unsigned OFF_KV = OFF_ST2 + 65536;
constexpr unsigned LAYER_B = 6u << 20;
constexpr unsigned OFF_O = OFF_KV + (4u << 20);
constexpr unsigned WS_USED = OFF_KV + 2 * LAYER_B;
constexpr unsigned SENT = 0xffffffffu;
constexpr unsigned SPIN_LIM = 4096;

#define STAMP_DECL do {} while (0)
#define STAMP(ws, slot) do {} while (0)
#define STAMP_FLUSH(ws, lo, hi) do {} while (0)

__device__ __forceinline__ float bflo(unsigned u) { return __uint_as_float(u << 16); }
__device__ __forceinline__ float bfhi(unsigned u) { return __uint_as_float(u & 0xffff0000u); }
__device__ __forceinline__ unsigned pk2(float a, float b) {
  f32x2 v = {a, b};
  bf16x2_t r = __builtin_convertvector(v, bf16x2_t);
  return __builtin_bit_cast(unsigned, r);
}
__device__ __forceinline__ float rbf(float a) { return bflo(pk2(a, a)); }
__device__ __forceinline__ f32x4 mfma(v4u a, v4u b, f32x4 c) {
  return __builtin_amdgcn_mfma_f32_16x16x32_bf16(__builtin_bit_cast(bf16x8, a), __builtin_bit_cast(bf16x8, b), c, 0, 0, 0);
}
template <int CTRL>
__device__ __forceinline__ float dpp(float x) {
  return __builtin_bit_cast(float, __builtin_amdgcn_mov_dpp(__builtin_bit_cast(int, x), CTRL, 0xf, 0xf, true));
}
__device__ __forceinline__ float xrow_sum(float x) {
  auto s = __builtin_amdgcn_permlane16_swap(__float_as_uint(x), __float_as_uint(x), false, false);
  x = __uint_as_float(s[0]) + __uint_as_float(s[1]);
  auto t = __builtin_amdgcn_permlane32_swap(__float_as_uint(x), __float_as_uint(x), false, false);
  return __uint_as_float(t[0]) + __uint_as_float(t[1]);
}
__device__ __forceinline__ float xrow_max(float x) {
  auto s = __builtin_amdgcn_permlane16_swap(__float_as_uint(x), __float_as_uint(x), false, false);
  x = __builtin_elementwise_maximum(__uint_as_float(s[0]), __uint_as_float(s[1]));
  auto t = __builtin_amdgcn_permlane32_swap(__float_as_uint(x), __float_as_uint(x), false, false);
  return __builtin_elementwise_maximum(__uint_as_float(t[0]), __uint_as_float(t[1]));
}
__device__ __forceinline__ float wave_sum(float x) {
  x += dpp<0xB1>(x);
  x += dpp<0x4E>(x);
  x += dpp<0x141>(x);
  x += dpp<0x128>(x);
  return xrow_sum(x);
}
__device__ __forceinline__ void glds16(const void* gsrc, unsigned lds_dst) {
  unsigned keep;
  asm volatile("s_mov_b32 %0, m0\n\ts_mov_b32 m0, %2\n\ts_nop 0\n\tglobal_load_lds_dwordx4 %1, off\n\ts_mov_b32 m0, %0"
               : "=&s"(keep) : "v"(gsrc), "s"(lds_dst) : "memory");
}
__device__ __forceinline__ v2u trread(lptr p) {
  return __builtin_bit_cast(v2u, __builtin_amdgcn_ds_read_tr16_b64_v4i16((LAS v4i16*)p));
}
__device__ __forceinline__ void bar() {
  asm volatile("s_waitcnt lgkmcnt(0)\n\ts_barrier" ::: "memory");
}
__device__ __forceinline__ void ln_stats(float sum, float sumsq, float& mb, float& sd) {
  const float mean = sum * (1.0f / D);
  const float var = fmaxf(fmaf(-mean, mean, sumsq * (1.0f / D)), 0.f);
  mb = rbf(mean);
  sd = rbf(__builtin_amdgcn_sqrtf(rbf(rbf(var) + 1e-5f)));
}
__device__ __forceinline__ unsigned gate_exact(unsigned x, unsigned y, float m0, float r0, float m1, float r1) {
  const float x0 = bflo(x), x1 = bfhi(x);
  unsigned p = pk2(x0 - m0, x1 - m1);
  p = pk2(bflo(p) * r0, bfhi(p) * r1);
  p = pk2(bflo(p) * bflo(y), bfhi(p) * bfhi(y));
  return pk2(x0 + bflo(p), x1 + bfhi(p));
}

constexpr int P_LDS = 44 * 1024;
extern "C" __global__ void __launch_bounds__(512) k_prep(const u16* __restrict__ x0, const u16* __restrict__ kin,
                                              const u16* __restrict__ vin, const u16* __restrict__ shk,
                                              const u16* __restrict__ wqkv, unsigned char* __restrict__ ws) {
  __shared__ __attribute__((aligned(16))) unsigned char smem[P_LDS];
  STAMP_DECL;
  STAMP(ws, 0);
  const lptr sm = (lptr)smem;
  const unsigned lb = (unsigned)(uintptr_t)sm;
  const int b = blockIdx.x, tid = threadIdx.x, lane = tid & 63;
  const int w = __builtin_amdgcn_readfirstlane(tid >> 6);
  const int cg = b & 7, tb = b >> 3;
  const __amdgpu_buffer_rsrc_t rs = __builtin_amdgcn_make_buffer_rsrc((void*)ws, 0, (int)WS_USED, 0x00020000);
  const int c = lane & 15, g = lane >> 4;
  v2u sw = {0u, 0u};
  if (cg < 7) {
    const int r8 = lane >> 3, ck = lane & 7;
    const size_t colb = (size_t)cg * 64;
    {
      const size_t t = (size_t)tb * 64 + w * 8 + r8;
      const int fk = (r8 & 2) | (((w + 1) & 1) << 2), fv = (r8 & 2) | ((w & 1) << 2);
      glds16(kin + t * D + colb + ((ck ^ fk) << 3), lb + (w + 1) * 1024);
      glds16(vin + t * D + colb + ((ck ^ fv) << 3), lb + 9216 + w * 1024);
      glds16(x0 + t * D + colb + ((ck ^ r8) << 3), lb + 17408 + w * 1024);
    }
    if (w == 0) {
      if (tb > 0) glds16(kin + ((size_t)tb * 64 - 8 + r8) * D + colb + ((ck ^ (r8 & 2)) << 3), lb);
      else *(LAS v4u*)(sm + lane * 16) = v4u{0u, 0u, 0u, 0u};
    }
    sw = *(const v2u*)(shk + (cg * 64 + (w & 3) * 16 + c) * 4);
  } else if (tb < 28) {
    const unsigned char* src = (const unsigned char*)wqkv + (size_t)tb * 43008 + lane * 16;
#pragma unroll
    for (int i = 0; i < 6; ++i) {
      const int pc = (i < 5 || w < 2) ? w + 8 * i : w;
      glds16(src + pc * 1024, lb + pc * 1024);
    }
  }
  const int srow = b * 8 + w;
  v4u sx = v4u{0u, 0u, 0u, 0u};
  if (lane < 56) sx = *(const v4u*)(x0 + (size_t)srow * D + lane * 8);
  asm volatile("s_waitcnt vmcnt(0)" ::: "memory");
  bar();
  STAMP(ws, 1);
  if (cg < 7) {
    const int q = (lane >> 2) & 3, p = lane & 3;
    const int s = (w >> 2) * 4 + g, ch16 = w & 3;
    const int chunk = 2 * ch16 + (p >> 1);
    const int a0 = (8 * s + q) * 128 + ((chunk ^ (q & 2) ^ ((s & 1) << 2)) << 4) + (p & 1) * 8;
    const int a1 = a0 ^ 64;
    const v2u k0 = trread(sm + a0 + 512);
    const v2u k1 = trread(sm + a1 + 1024);
    const v2u k2 = trread(sm + a1 + 1536);
    const v2u v0 = trread(sm + 9216 + a0);
    const v2u v1 = trread(sm + 9216 + a0 + 512);
    const int cpl = g ^ ((c >> 3) << 1);
    v4u xo;
#pragma unroll
    for (int j = 0; j < 4; ++j) xo[j] = *(LAS unsigned*)(sm + 17408 + (16 * j + c) * 128 + ((w ^ (c & 7)) << 4) + cpl * 4);
    float kk[12] = {bflo(k0.x), bfhi(k0.x), bflo(k0.y), bfhi(k0.y), bflo(k1.x), bfhi(k1.x),
                    bflo(k1.y), bfhi(k1.y), bflo(k2.x), bfhi(k2.x), bflo(k2.y), bfhi(k2.y)};
    const unsigned vd[4] = {v0.x, v0.y, v1.x, v1.y};
    const float w0 = bflo(sw.x), w1 = bfhi(sw.x), w2 = bflo(sw.y), w3 = bfhi(sw.y);
    v4u o;
#pragma unroll
    for (int i = 0; i < 4; ++i) {
      const int e = 2 * i + 4;
      unsigned a = pk2(kk[e - 3] * w3, kk[e - 2] * w3);
      unsigned m = pk2(kk[e - 2] * w2, kk[e - 1] * w2);
      a = pk2(bflo(a) + bflo(m), bfhi(a) + bfhi(m));
      m = pk2(kk[e - 1] * w1, kk[e] * w1);
      a = pk2(bflo(a) + bflo(m), bfhi(a) + bfhi(m));
      m = pk2(kk[e] * w0, kk[e + 1] * w0);
      a = pk2(bflo(a) + bflo(m), bfhi(a) + bfhi(m));
      o[3 - i] = pk2(bfhi(a) * bfhi(vd[i]), bflo(a) * bflo(vd[i]));
    }
    *(v4u*)(ws + OFF_KVR + ((size_t)(cg * 64 + ch16 * 16 + c) * S + (S - 8 - tb * 64 - 8 * s)) * 2) = o;
    *(v4u*)(ws + OFF_X0P + ((size_t)(cg * 32 + w * 4 + cpl) * 32 + tb) * 256 + c * 16) = xo;
  } else if (tb < 28) {
    const int q = (lane >> 2) & 3, p = lane & 3;
    const int half = g >> 1, gk = g & 1;
    const int abase = (8 * gk + q) * 2688 + half * 32 + p * 8;
#pragma unroll
    for (int i = 0; i < 6; ++i) {
      const int u = w * 6 + i, h = u / 3, sec = u - h * 3;
      const int col = (sec == 2 ? 0 : (sec + 1) * D) + h * DH;
      const v2u lo = trread(sm + abase + col * 2);
      const v2u hi = trread(sm + abase + col * 2 + 4 * 2688);
      v4u o = {lo.x, lo.y, hi.x, hi.y};
      if (half && c >= 12) {
        const unsigned f = (sec == 0 && c == 12) ? 0x3F803F80u : 0u;
        o = v4u{f, f, f, f};
      }
      *(v4u*)(ws + OFF_WQF + h * WQ_HEAD + (1 + (sec * 14 + (tb >> 1)) * 2 + half) * 1024 + (((tb & 1) * 2 + gk) * 16 + c) * 16) = o;
    }
  }
  {
    float s = 0.f, q = 0.f;
#pragma unroll
    for (int j = 0; j < 4; ++j) {
      const float d0 = bflo(sx[j]), d1 = bfhi(sx[j]);
      s += d0 + d1;
      q = fmaf(d0, d0, q);
      q = fmaf(d1, d1, q);
    }
    s = wave_sum(s);
    q = wave_sum(q);
    float mb, sd;
    ln_stats(s, q, mb, sd);
    if (lane == 0) *(unsigned*)(ws + OFF_ST0 + (srow >> 6) * 256 + (srow & 15) * 16 + ((srow >> 4) & 3) * 4) = pk2(mb, sd);
  }
  if (tb == 1) {
    unsigned long long pc;
    unsigned len, dummy, rel;
    asm volatile("s_getpc_b64 %0\npf_anchor:\n\ts_mov_b32 %1, mega_code_end-pf_anchor\n\ts_mov_b32 %2, k_conv-pf_anchor"
                 : "=s"(pc), "=s"(len), "=s"(rel));
    const unsigned off = rel + tid * 128;
    if (off < len) asm volatile("global_load_dword %0, %1, %2\n\ts_waitcnt vmcnt(0)" : "=v"(dummy) : "v"(off), "s"(pc) : "memory");
  }
  STAMP(ws, 2);
  STAMP_FLUSH(ws, 0, 2);
}

constexpr int CV_CS = 4160;
constexpr int CV_UR = 5120;
constexpr int CV_OFF_U = 8 * CV_CS;
constexpr int CV_OFF_R = CV_OFF_U + 2 * CV_UR;
constexpr int CV_LDS = CV_OFF_R + 32768;

extern "C" __global__ void __launch_bounds__(512) k_conv(const u16* __restrict__ ssmk, const u16* __restrict__ ssmD,
                                              const u16* __restrict__ wo, unsigned char* __restrict__ ws) {
  extern __shared__ __attribute__((aligned(16))) unsigned char smem[];
  STAMP_DECL;
  STAMP(ws, 3);
  const lptr sm = (lptr)smem;
  const unsigned lb = (unsigned)(uintptr_t)sm;
  const int b = blockIdx.x, tid = threadIdx.x, lane = tid & 63;
  const int w = __builtin_amdgcn_readfirstlane(tid >> 6);
  const int c = lane & 15, g = lane >> 4;
  const __amdgpu_buffer_rsrc_t rs = __builtin_amdgcn_make_buffer_rsrc((void*)ws, 0, (int)WS_USED, 0x00020000);
  const v4u sv = {SENT, SENT, SENT, SENT};
  if ((b & 7) == 7) {
    const int idx = b >> 3;
#pragma unroll
    for (int i = 0; i < 3; ++i)
      __builtin_amdgcn_raw_buffer_store_b128(sv, rs, OFF_KV + (i * 256 + b) * 8192 + tid * 16, 0, 16);
    if (idx < 16) {
      const unsigned char* src = (const unsigned char*)wo + (size_t)idx * 25088;
#pragma unroll
      for (int i = 0; i < 4; ++i) {
        const int pc = (i < 3 || w == 0) ? w + 8 * i : w;
        const int off = pc * 1024 + lane * 16;
        glds16(src + (off < 25088 ? off : 0), lb + pc * 1024);
      }
      asm volatile("s_waitcnt vmcnt(0)" ::: "memory");
      bar();
      const int q = (lane >> 2) & 3, p = lane & 3;
      const int alo = (4 * g + q) * 896 + p * 8;
      const int ahi = alo + (g < 3 ? 16 * 896 : 0);
#pragma unroll
      for (int i = 0; i < 4; ++i) {
        const int ct = (i < 3 || w < 4) ? w + 8 * i : w;
        const v2u lo = trread(sm + alo + ct * 32);
        v2u hi = trread(sm + ahi + ct * 32);
        if (g == 3) hi = v2u{0u, 0u};
        *(v4u*)(ws + OFF_WOF + (ct * 16 + idx) * 1024 + lane * 16) = v4u{lo.x, lo.y, hi.x, hi.y};
      }
    } else if (w < 6) {
      unsigned char* hb = ws + OFF_WQF + (idx - 16) * WQ_HEAD;
      float s = 0.f;
      v4u f[14];
#pragma unroll
      for (int ks = 0; ks < 14; ++ks) f[ks] = *(const v4u*)(hb + (1 + ((w >> 1) * 14 + ks) * 2 + (w & 1)) * 1024 + lane * 16);
#pragma unroll
      for (int ks = 0; ks < 14; ++ks)
#pragma unroll
        for (int j = 0; j < 4; ++j) s += bflo(f[ks][j]) + bfhi(f[ks][j]);
      s = xrow_sum(s);
      if (lane < 16) ((float*)hb)[w * 16 + lane] = s;
    }
    STAMP(ws, 4); STAMP(ws, 5); STAMP(ws, 6);
    STAMP_FLUSH(ws, 3, 6);
    return;
  }
  const int cp = (b & 7) * 32 + (b >> 3);
  const int qs = w >> 2, mych = (w >> 1) & 1, kh = w & 1;
  const int qf = 2 * (w & 3) + 1 - qs;
  glds16(ws + OFF_KVR + (size_t)cp * 8192 + tid * 16, lb + CV_OFF_U + (w >> 2) * CV_UR + (w & 3) * 1024);
  const int hch = tid >> 8, hck = tid & 255;
  const u16* hp = ssmk + (size_t)cp * 4096 + tid * 8;
  const v4u hd = *(const v4u*)hp;
  v2u hn = *(const v2u*)(hp + (hck < 255 ? 8 : 0));
  const v4u x0v = *(const v4u*)(ws + OFF_X0P + ((size_t)cp * 32 + qf * 4) * 256 + lane * 16);
  const v4u stv = *(const v4u*)(ws + OFF_ST0 + qf * 1024 + lane * 16);
  const unsigned dD = *(const unsigned*)(ssmD + 2 * cp);
  if (tid < 128) *(LAS v4u*)(sm + CV_OFF_U + (tid >> 6) * CV_UR + 4096 + lane * 16) = v4u{0u, 0u, 0u, 0u};
  {
    if (hck == 255) hn = v2u{0u, 0u};
    const unsigned d0 = hd.x, d1 = hd.y, d2 = hd.z, d3 = hd.w, d4 = hn.x, d5 = hn.y;
    const unsigned a01 = __builtin_amdgcn_alignbit(d1, d0, 16), a12 = __builtin_amdgcn_alignbit(d2, d1, 16);
    const unsigned a23 = __builtin_amdgcn_alignbit(d3, d2, 16), a34 = __builtin_amdgcn_alignbit(d4, d3, 16);
    const unsigned a45 = __builtin_amdgcn_alignbit(d5, d4, 16);
    const lptr base = sm + hch * 4 * CV_CS + 32 + hck * 16;
    *(LAS v4u*)(base + 0 * CV_CS) = v4u{a01, a12, a23, a34};
    *(LAS v4u*)(base + 1 * CV_CS) = v4u{d1, d2, d3, d4};
    *(LAS v4u*)(base + 2 * CV_CS) = v4u{a12, a23, a34, a45};
    *(LAS v4u*)(base + 3 * CV_CS) = v4u{d2, d3, d4, d5};
    if (hck == 0) {
      const lptr bz = sm + hch * 4 * CV_CS;
      const v4u z = {0u, 0u, 0u, 0u};
      *(LAS v4u*)(bz + 0 * CV_CS) = z;
      *(LAS v4u*)(bz + 1 * CV_CS) = z;
      *(LAS v4u*)(bz + 2 * CV_CS) = z;
      *(LAS v4u*)(bz + 3 * CV_CS) = z;
      *(LAS v4u*)(bz + 0 * CV_CS + 16) = v4u{0u, 0u, 0u, d0 << 16};
      *(LAS v4u*)(bz + 1 * CV_CS + 16) = v4u{0u, 0u, 0u, d0};
      *(LAS v4u*)(bz + 2 * CV_CS + 16) = v4u{0u, 0u, d0 << 16, a01};
      *(LAS v4u*)(bz + 3 * CV_CS + 16) = v4u{0u, 0u, d0, d1};
    }
  }
  bar();
  STAMP(ws, 4);
#pragma unroll
  for (int i = 0; i < 3; ++i)
    __builtin_amdgcn_raw_buffer_store_b128(sv, rs, OFF_KV + (i * 256 + b) * 8192 + tid * 16, 0, 16);
  const lptr abase = sm + mych * 4 * CV_CS + (c & 3) * CV_CS + ((c & 12) + 8 * g) * 2 + kh * 256;
  const lptr bbase = sm + CV_OFF_U + mych * CV_UR + (2032 - 16 * c + 8 * g) * 2 + kh * 256 + qs * 512 - 7 * 512;
  f32x4 acc[4];
#pragma unroll
  for (int i = 0; i < 4; ++i) acc[i] = f32x4{0.f, 0.f, 0.f, 0.f};
  v4u A[2][8], U[2][8];
#define CV_LOAD(buf, kp)                                                                  \
  _Pragma("unroll") for (int p = 0; p < 8; ++p) {                                         \
    U[buf][7 - p] = *(const LAS v4u*)(bbase + p * 512 + (kp) * 64);                        \
    const v2u lo = *(const volatile LAS v2u*)(abase + p * 512 + (kp) * 64);                \
    const v2u hi = *(const volatile LAS v2u*)(abase + p * 512 + (kp) * 64 + 8);            \
    A[buf][p] = v4u{lo.x, lo.y, hi.x, hi.y};                                              \
  }
  CV_LOAD(0, 0);
  u16 ur[2][4];
#pragma unroll
  for (int ch = 0; ch < 2; ++ch)
#pragma unroll
    for (int j = 0; j < 4; ++j) ur[ch][j] = *(const LAS u16*)(sm + CV_OFF_U + ch * CV_UR + (2047 - (256 * qf + 64 * g + 16 * j + c)) * 2);
#pragma unroll
  for (int kp = 0; kp < 4; ++kp) {
    if (kp < 3) { CV_LOAD((kp + 1) & 1, kp + 1); }
    __builtin_amdgcn_sched_barrier(0);
#pragma unroll
    for (int p = 0; p < 7; ++p)
#pragma unroll
      for (int i = 3; i >= 0; --i)
        if (2 * i + 1 > p) acc[i] = mfma(U[kp & 1][2 * i + 1 - p], A[kp & 1][p], acc[i]);
    if (qs == 0) {
#pragma unroll
      for (int i = 0; i < 4; ++i) acc[i] = mfma(U[kp & 1][0], A[kp & 1][2 * i + 1], acc[i]);
    }
    __builtin_amdgcn_sched_barrier(0);
  }
  STAMP(ws, 5);
  {
    const lptr rw = sm + CV_OFF_R + qs * 16384 + (w & 3) * 1024 + lane * 16;
#pragma unroll
    for (int i = 0; i < 4; ++i) *(LAS f32x4*)(rw + i * 4096) = acc[i];
  }
  unsigned kd[4];
  float mb[4], rsd[4];
  const float D0 = bflo(dD), D1 = bfhi(dD);
#pragma unroll
  for (int j = 0; j < 4; ++j) {
    kd[j] = pk2(bflo(ur[0][j]) * D0, bflo(ur[1][j]) * D1);
    mb[j] = bflo(stv[j]);
    rsd[j] = __builtin_amdgcn_rcpf(bfhi(stv[j]));
  }
  bar();
  {
    const lptr rr = sm + CV_OFF_R + w * 4096 + lane * 16;
    const f32x4 s0 = *(const LAS f32x4*)(rr) + *(const LAS f32x4*)(rr + 1024);
    const f32x4 s1 = *(const LAS f32x4*)(rr + 2048) + *(const LAS f32x4*)(rr + 3072);
    const size_t obase = ((size_t)(cp >> 2) * S + 256 * qf + 64 * g + c) * 16 + (cp & 3) * 4;
#pragma unroll
    for (int j = 0; j < 4; ++j) {
      unsigned y = pk2(s0[j], s1[j]);
      y = pk2(bflo(y) + bflo(kd[j]), bfhi(y) + bfhi(kd[j]));
      *(unsigned*)(ws + OFF_YF + obase + j * 256) = y;
      *(unsigned*)(ws + OFF_X1F + obase + j * 256) = gate_exact(x0v[j], y, mb[j], rsd[j], mb[j], rsd[j]);
    }
  }
  STAMP(ws, 6);
  STAMP_FLUSH(ws, 3, 6);
}

constexpr int M_OFF_V = WQ_HEAD;
constexpr int M_OFF_K = M_OFF_V + 24 * 1024;
constexpr int M_OFF_S = M_OFF_K + 32 * 1024;
constexpr int M_S_LN = M_OFF_S;
constexpr int M_S_G = M_OFF_S + 1024;
constexpr int M_S_P = M_OFF_S + 2048;
constexpr int M_LDS = M_OFF_S + 4096;

#define DLY_O 12
#define DLY_X 0

#define LANE()                                                                            \
  unsigned lane = __builtin_amdgcn_mbcnt_hi(~0u, __builtin_amdgcn_mbcnt_lo(~0u, 0u));     \
  asm volatile("" : "+v"(lane));                                                          \
  __builtin_assume(lane < 64u);                                                           \
  const unsigned c = lane & 15, g = lane >> 4;                                            \
  int w = w_, b = b_;                                                                     \
  asm volatile("" : "+s"(w), "+s"(b));                                                    \
  const int ti = (b & 7) * 2 + (b >> 7), h = (b >> 3) & 15;                               \
  const int rb = h >> 2, cq = h & 3;                                                      \
  const int ct = cq * 7 + (w < 7 ? w : 6);                                                \
  const unsigned wsrc = OFF_WQF + h * WQ_HEAD;                                            \
  const unsigned kvbase = OFF_KV + layer * LAYER_B + (ti * 16 + h) * 16384 + w * 1024; \
  const unsigned nbo0 = kvbase - (ti > 0 ? 16 * 16384 : 0);                               \
  const unsigned nbo1 = kvbase - (ti > 1 ? 32 * 16384 : 0);                               \
  const unsigned obase = OFF_O + layer * LAYER_B + ti * (8 * 16384);                 \
  (void)c; (void)g; (void)rb; (void)ct; (void)wsrc; (void)nbo0; (void)nbo1; (void)obase
__device__ __forceinline__ void blds16(__amdgpu_buffer_rsrc_t rs, unsigned voff, unsigned soff, unsigned lds_dst) {
  unsigned keep;
  asm volatile("s_mov_b32 %0, m0\n\ts_mov_b32 m0, %4\n\ts_nop 0\n\tbuffer_load_dwordx4 %1, %2, %3 offen lds\n\ts_mov_b32 m0, %0"
               : "=&s"(keep) : "v"(voff), "s"(rs), "s"(soff), "s"(lds_dst) : "memory");
}
__device__ __forceinline__ unsigned smax(v4u v) { return max(v.x, v.z); }

extern "C" __global__ void __launch_bounds__(512, 2) k_mega(unsigned char* __restrict__ ws, u16* __restrict__ out) {
  extern __shared__ __attribute__((aligned(16))) unsigned char smem[];
  STAMP_DECL;
  STAMP(ws, 7);
  const lptr sm = (lptr)smem;
  const unsigned lb = (unsigned)(uintptr_t)sm;
  const int b_ = blockIdx.x;
  const int w_ = __builtin_amdgcn_readfirstlane(threadIdx.x >> 6);
  const __amdgpu_buffer_rsrc_t rs = __builtin_amdgcn_make_buffer_rsrc((void*)ws, 0, (int)WS_USED, 0x00020000);
  v4u X[14], Y[14];
  v4u xres = {0u, 0u, 0u, 0u}, yres = {0u, 0u, 0u, 0u};
#pragma unroll
  for (int ks = 0; ks < 14; ++ks) asm volatile("" : "=v"(Y[ks]));

#pragma nounroll
  for (int layer = 0; layer < 2; ++layer) {
    __builtin_amdgcn_s_waitcnt(0x0F70);
    float ga = 0.f, gb = 0.f;
    if (layer == 0) {
      LANE();
#pragma unroll
      for (int i = 0; i < 4; ++i) blds16(rs, lane * 16, wsrc + (w + 8 * i) * 1024, lb + (w + 8 * i) * 1024);
    } else {
      LANE();
      __builtin_amdgcn_s_sleep(DLY_X);
      v2u st;
      for (unsigned spins = 0;; ++spins) {
        asm volatile("" ::: "memory");
        st = __builtin_amdgcn_raw_buffer_load_b64(rs, (c * 4 + g) * 8, OFF_ST2 + (ti * 128 + w * 16) * 32, 16);
        if (!__any(st.x == SENT) || spins > SPIN_LIM) break;
        __builtin_amdgcn_s_sleep(2);
      }
      float mb, sd;
      ln_stats(xrow_sum(__uint_as_float(st.x)), xrow_sum(__uint_as_float(st.y)), mb, sd);
      ga = __builtin_amdgcn_rcpf(sd);
      gb = -mb * ga;
      if (g == 0) *(LAS f32x2*)(sm + M_S_G + (w * 16 + c) * 8) = f32x2{mb, ga};
    }
    STAMP(ws, 8 + layer * 12);
    f32x4 ak0, ak1, gram;
    for (unsigned spins = 0;; ++spins) {
      LANE();
      {
        const unsigned xf_off = (g * S + c) * 16;
        unsigned so = (layer ? OFF_X2F : OFF_X1F) + (ti * 128 + w * 16) * 16;
#pragma unroll
        for (int ks = 0; ks < 14; ++ks)
          asm volatile("buffer_load_dwordx4 %0, %2, %3, %1 offen sc1\n\ts_add_u32 %1, %1, 0x20000" : "=v"(X[ks]), "+s"(so) : "v"(xf_off), "s"(rs) : "memory", "scc");
      }
      if (layer == 0) {
        if (ti < 2) {
#pragma unroll
          for (int i = 0; i < 2; ++i) *(LAS v4u*)(sm + M_OFF_V + i * 8192 + w * 1024 + lane * 16) = v4u{0u, 0u, 0u, 0u};
        }
        asm volatile("s_waitcnt vmcnt(14)\n\ts_barrier" ::: "memory");
#pragma unroll
        for (int i = 4; i < 11; ++i) {
          const int pc = (i < 10 || w < 5) ? w + 8 * i : w;
          blds16(rs, lane * 16, wsrc + pc * 1024, lb + pc * 1024);
        }
        const v4u sv = {SENT, SENT, SENT, SENT};
        __builtin_amdgcn_raw_buffer_store_b128(sv, rs, lane * 16, kvbase + LAYER_B, 16);
        __builtin_amdgcn_raw_buffer_store_b128(sv, rs, lane * 16, kvbase + LAYER_B + 8192, 16);
        __builtin_amdgcn_raw_buffer_store_b128(sv, rs, lane * 16, obase + LAYER_B + (w * 16 + h) * 1024, 16);
        if (w < 7) __builtin_amdgcn_raw_buffer_store_b128(sv, rs, ((g >> 1) * S + (lane & 31)) * 16, OFF_X2F + (ct * 2 * S + ti * 128 + rb * 32) * 16, 16);
        else __builtin_amdgcn_raw_buffer_store_b64(v2u{SENT, SENT}, rs, (lane & 31) * 32, OFF_ST2 + ((ti * 128 + rb * 32) * 4 + cq) * 8, 16);
        asm volatile("" ::: "memory");
      }
      const lptr wl = sm + 1024 + lane * 16;
      ak0 = ak1 = gram = f32x4{0.f, 0.f, 0.f, 0.f};
      unsigned bad = 0u;
      v4u wa[3][2];
#pragma unroll
      for (int i = 0; i < 2; ++i) {
        wa[i][0] = *(const LAS v4u*)(wl + (i * 2) * 1024);
        wa[i][1] = *(const LAS v4u*)(wl + (i * 2 + 1) * 1024);
      }
#pragma unroll
      for (int ks = 0; ks < 14; ++ks) {
        if (ks + 2 < 14) {
          wa[(ks + 2) % 3][0] = *(const LAS v4u*)(wl + ((ks + 2) * 2) * 1024);
          wa[(ks + 2) % 3][1] = *(const LAS v4u*)(wl + ((ks + 2) * 2 + 1) * 1024);
        }
        asm volatile("s_cmp_eq_u32 %2, 0\n\ts_cbranch_scc1 1f\n\ts_waitcnt vmcnt(%3)\n1:\n\ts_waitcnt vmcnt(%4)"
                     : "+v"(X[ks]), "+v"(Y[ks]) : "s"(layer), "i"(13 - ks), "i"(24 - ks) : "scc");
        if (layer) {
          bad = max(bad, smax(X[ks]));
          asm volatile("" : "+v"(bad));
#pragma unroll
          for (int j = 0; j < 4; ++j) {
            const float x0 = bflo(X[ks][j]), x1 = bfhi(X[ks][j]);
            X[ks][j] = pk2(fmaf(fmaf(x0, ga, gb), bflo(Y[ks][j]), x0), fmaf(fmaf(x1, ga, gb), bfhi(Y[ks][j]), x1));
          }
        }
        ak1 = mfma(wa[ks % 3][1], X[ks], ak1);
        gram = mfma(X[ks], X[ks], gram);
        ak0 = mfma(wa[ks % 3][0], X[ks], ak0);
        __builtin_amdgcn_sched_barrier(0);
      }
      if (layer == 0 || !__any(bad == SENT) || spins > SPIN_LIM) break;
    }
    STAMP(ws, 9 + layer * 12);
    float mean, rstd;
    {
      LANE();
      const f32x4 cs0 = *(const LAS f32x4*)(sm + g * 16), cs1 = *(const LAS f32x4*)(sm + 64 + g * 16);
      const float g01 = (c & 1) ? gram[1] : gram[0], g23 = (c & 1) ? gram[3] : gram[2];
      const float gsel = (c & 2) ? g23 : g01;
      float sd;
      ln_stats(xrow_sum(g == 3 ? ak1[0] : 0.f), xrow_sum(g == (c >> 2) ? gsel : 0.f), mean, sd);
      rstd = __builtin_amdgcn_rcpf(sd);
      v4u kf;
      kf.x = pk2((ak0[0] - mean * cs0[0]) * rstd, (ak0[1] - mean * cs0[1]) * rstd);
      kf.y = pk2((ak0[2] - mean * cs0[2]) * rstd, (ak0[3] - mean * cs0[3]) * rstd);
      kf.z = pk2((ak1[0] - mean * cs1[0]) * rstd, (ak1[1] - mean * cs1[1]) * rstd);
      kf.w = pk2((ak1[2] - mean * cs1[2]) * rstd, (ak1[3] - mean * cs1[3]) * rstd);
      __builtin_amdgcn_raw_buffer_store_b128(kf, rs, lane * 16, kvbase, 16);
      *(LAS v4u*)(sm + M_OFF_K + 16384 + w * 1024 + lane * 16) = kf;
      *(LAS f32x2*)(sm + M_S_LN + (w * 16 + c) * 8) = f32x2{mean, rstd};
    }
    if (layer == 0) asm volatile("s_waitcnt vmcnt(1)\n\ts_barrier" ::: "memory");
    STAMP(ws, 10 + layer * 12);
    v4u nb0, nb1;
    {
      LANE();
      const lptr wl = sm + 29 * 1024 + lane * 16;
      f32x4 a0 = {0.f, 0.f, 0.f, 0.f}, a1 = {0.f, 0.f, 0.f, 0.f};
      v4u wa[4][2];
#pragma unroll
      for (int i = 0; i < 3; ++i) {
        wa[i][0] = *(const LAS v4u*)(wl + (i * 2) * 1024);
        wa[i][1] = *(const LAS v4u*)(wl + (i * 2 + 1) * 1024);
      }
      const float cs0 = *(const LAS float*)(sm + 128 + c * 4), cs1 = *(const LAS float*)(sm + 192 + c * 4);
      const f32x4 s01 = *(const LAS f32x4*)(sm + M_S_LN + (w * 16 + 4 * g) * 8);
      const f32x4 s23 = *(const LAS f32x4*)(sm + M_S_LN + (w * 16 + 4 * g) * 8 + 16);
#pragma unroll
      for (int ks = 0; ks < 14; ++ks) {
        if (ks + 3 < 14) {
          wa[(ks + 3) & 3][0] = *(const LAS v4u*)(wl + ((ks + 3) * 2) * 1024);
          wa[(ks + 3) & 3][1] = *(const LAS v4u*)(wl + ((ks + 3) * 2 + 1) * 1024);
        }
        a0 = mfma(X[ks], wa[ks & 3][0], a0);
        a1 = mfma(X[ks], wa[ks & 3][1], a1);
        __builtin_amdgcn_sched_barrier(0);
      }
      nb0 = __builtin_amdgcn_raw_buffer_load_b128(rs, lane * 16, nbo0, 16);
      nb1 = __builtin_amdgcn_raw_buffer_load_b128(rs, lane * 16, nbo1, 16);
      v4u vf;
      vf.x = pk2((a0[0] - s01[0] * cs0) * s01[1], (a0[1] - s01[2] * cs0) * s01[3]);
      vf.y = pk2((a0[2] - s23[0] * cs0) * s23[1], (a0[3] - s23[2] * cs0) * s23[3]);
      vf.z = pk2((a1[0] - s01[0] * cs1) * s01[1], (a1[1] - s01[2] * cs1) * s01[3]);
      vf.w = pk2((a1[2] - s23[0] * cs1) * s23[1], (a1[3] - s23[2] * cs1) * s23[3]);
      if (c == 12) { vf.z = 0x3F803F80u; vf.w = 0x3F803F80u; }
      __builtin_amdgcn_raw_buffer_store_b128(vf, rs, lane * 16, kvbase + 8192, 16);
      *(LAS v2u*)(sm + M_OFF_V + (16 + w) * 1024 + lane * 8) = v2u{vf.x, vf.y};
      *(LAS v2u*)(sm + M_OFF_V + (16 + w) * 1024 + 512 + lane * 8) = v2u{vf.z, vf.w};
    }
    STAMP(ws, 11 + layer * 12);
    v4u qf;
    {
      LANE();
      const lptr wl = sm + 57 * 1024 + lane * 16;
      f32x4 a0 = {0.f, 0.f, 0.f, 0.f}, a1 = {0.f, 0.f, 0.f, 0.f};
      v4u wa[4][2];
#pragma unroll
      for (int i = 0; i < 3; ++i) {
        wa[i][0] = *(const LAS v4u*)(wl + (i * 2) * 1024);
        wa[i][1] = *(const LAS v4u*)(wl + (i * 2 + 1) * 1024);
      }
      const f32x4 cs0 = *(const LAS f32x4*)(sm + 256 + g * 16), cs1 = *(const LAS f32x4*)(sm + 320 + g * 16);
#pragma unroll
      for (int ks = 0; ks < 14; ++ks) {
        if (ks + 3 < 14) {
          wa[(ks + 3) & 3][0] = *(const LAS v4u*)(wl + ((ks + 3) * 2) * 1024);
          wa[(ks + 3) & 3][1] = *(const LAS v4u*)(wl + ((ks + 3) * 2 + 1) * 1024);
        }
        a0 = mfma(wa[ks & 3][0], X[ks], a0);
        a1 = mfma(wa[ks & 3][1], X[ks], a1);
        __builtin_amdgcn_sched_barrier(0);
      }
      const float qsc = rstd * (0.18898223650461361f * 1.4426950408889634f);
      qf.x = pk2((a0[0] - mean * cs0[0]) * qsc, (a0[1] - mean * cs0[1]) * qsc);
      qf.y = pk2((a0[2] - mean * cs0[2]) * qsc, (a0[3] - mean * cs0[3]) * qsc);
      qf.z = pk2((a1[0] - mean * cs1[0]) * qsc, (a1[1] - mean * cs1[1]) * qsc);
      qf.w = pk2((a1[2] - mean * cs1[2]) * qsc, (a1[3] - mean * cs1[3]) * qsc);
      if (g == 3) qf.z = 0x3F800000u;
      asm volatile("" : "+v"(qf));
    }
    STAMP(ws, 12 + layer * 12);
    v4u WO[16];
    {
      LANE();
      if (__any(max(smax(nb0), smax(nb1)) == SENT)) {
        for (unsigned spins = 0;; ++spins) {
          asm volatile("" ::: "memory");
          nb0 = __builtin_amdgcn_raw_buffer_load_b128(rs, lane * 16, nbo0, 16);
          nb1 = __builtin_amdgcn_raw_buffer_load_b128(rs, lane * 16, nbo1, 16);
          if (!__any(max(smax(nb0), smax(nb1)) == SENT) || spins > SPIN_LIM) break;
        }
      }
    }
    {
      LANE();
      if (ti < 2) {
        const v4u none = {0u, 0u, g == 3 ? 0xF1490000u : 0u, 0u};
        nb1 = none;
        if (ti == 0) nb0 = none;
      }
      *(LAS v4u*)(sm + M_OFF_K + 8192 + w * 1024 + lane * 16) = nb0;
      *(LAS v4u*)(sm + M_OFF_K + w * 1024 + lane * 16) = nb1;
      asm volatile("" ::: "memory");
      nb0 = __builtin_amdgcn_raw_buffer_load_b128(rs, lane * 16, nbo0 + 8192, 16);
      nb1 = __builtin_amdgcn_raw_buffer_load_b128(rs, lane * 16, nbo1 + 8192, 16);
      bar();
    }
    STAMP(ws, 13 + layer * 12);
    v4u kfr[17];
    f32x4 cm, cm0, cm16;
    {
      LANE();
      const lptr kl = sm + M_OFF_K + w * 1024 + lane * 16;
#pragma unroll
      for (int n = 0; n < 17; ++n) kfr[n] = *(const LAS v4u*)(kl + n * 1024);
      const float NEG = -__builtin_inff();
#pragma unroll
      for (int j = 0; j < 4; ++j) {
        cm0[j] = (4 * g + j <= c) ? NEG : 0.f;
        cm16[j] = (4 * g + j > c) ? NEG : 0.f;
      }
      __builtin_amdgcn_sched_barrier(0);
      float mx0 = NEG, mx1 = NEG;
#pragma unroll
      for (int n = 0; n < 17; ++n) {
        const f32x4 sc = mfma(kfr[n], qf, n == 0 ? cm0 : (n == 16 ? cm16 : f32x4{0.f, 0.f, 0.f, 0.f}));
        mx0 = __builtin_elementwise_maximum(__builtin_elementwise_maximum(mx0, sc[0]), sc[1]);
        mx1 = __builtin_elementwise_maximum(__builtin_elementwise_maximum(mx1, sc[2]), sc[3]);
      }
      const float mx = xrow_max(__builtin_elementwise_maximum(mx0, mx1));
      cm = f32x4{-mx, -mx, -mx, -mx};
      cm0 += cm;
      cm16 += cm;
      asm volatile("" : "+v"(cm), "+v"(cm0), "+v"(cm16));
    }
    STAMP(ws, 14 + layer * 12);
    {
      LANE();
      if (__any(max(smax(nb0), smax(nb1)) == SENT)) {
        for (unsigned spins = 0;; ++spins) {
          asm volatile("" ::: "memory");
          nb0 = __builtin_amdgcn_raw_buffer_load_b128(rs, lane * 16, nbo0 + 8192, 16);
          nb1 = __builtin_amdgcn_raw_buffer_load_b128(rs, lane * 16, nbo1 + 8192, 16);
          if (!__any(max(smax(nb0), smax(nb1)) == SENT) || spins > SPIN_LIM) break;
        }
      }
    }
    {
      LANE();
      const lptr vl = sm + M_OFF_V + w * 1024 + lane * 8;
      if (ti > 0) {
        *(LAS v2u*)(vl + 8192) = v2u{nb0.x, nb0.y};
        *(LAS v2u*)(vl + 8192 + 512) = v2u{nb0.z, nb0.w};
      }
      if (ti > 1) {
        *(LAS v2u*)(vl) = v2u{nb1.x, nb1.y};
        *(LAS v2u*)(vl + 512) = v2u{nb1.z, nb1.w};
      }
      if (layer == 0) {
        const unsigned xo_off = ((g >> 1) * S + (lane & 31)) * 16;
        xres = __builtin_amdgcn_raw_buffer_load_b128(rs, xo_off, OFF_X1F + (ct * 2 * S + ti * 128 + rb * 32) * 16, 0);
        yres = __builtin_amdgcn_raw_buffer_load_b128(rs, xo_off, OFF_YF + (ct * 2 * S + ti * 128 + rb * 32) * 16, 0);
      }
      f32x4 s0 = mfma(kfr[0], qf, cm0), s1 = mfma(kfr[1], qf, cm);
      bar();
      STAMP(ws, 15 + layer * 12);
      f32x4 o0 = {0.f, 0.f, 0.f, 0.f}, o1 = {0.f, 0.f, 0.f, 0.f};
#pragma unroll
      for (int i = 0; i < 9; ++i) {
        const int n = 2 * i, n1 = n + 1 < 17 ? n + 1 : n - 1;
        const v2u a0 = *(const volatile LAS v2u*)(vl + n * 1024), a1 = *(const volatile LAS v2u*)(vl + n1 * 1024);
        const v2u b0 = *(const volatile LAS v2u*)(vl + n * 1024 + 512), b1 = *(const volatile LAS v2u*)(vl + n1 * 1024 + 512);
        f32x4 t0 = s0, t1 = s1;
        if (i < 8) {
          s0 = mfma(kfr[n + 2], qf, i == 7 ? cm16 : cm);
          if (i < 7) s1 = mfma(kfr[n + 3], qf, cm);
          WO[n] = __builtin_amdgcn_raw_buffer_load_b128(rs, lane * 16 + (n & 3) * 1024, OFF_WOF + (ct * 16 + (n & 12)) * 1024, 0);
          WO[n + 1] = __builtin_amdgcn_raw_buffer_load_b128(rs, lane * 16 + ((n + 1) & 3) * 1024, OFF_WOF + (ct * 16 + (n & 12)) * 1024, 0);
        }
        v4u pf;
        pf.x = pk2(__builtin_amdgcn_exp2f(t0[0]), __builtin_amdgcn_exp2f(t0[1]));
        pf.y = pk2(__builtin_amdgcn_exp2f(t0[2]), __builtin_amdgcn_exp2f(t0[3]));
        if (i < 8) {
          pf.z = pk2(__builtin_amdgcn_exp2f(t1[0]), __builtin_amdgcn_exp2f(t1[1]));
          pf.w = pk2(__builtin_amdgcn_exp2f(t1[2]), __builtin_amdgcn_exp2f(t1[3]));
        } else {
          pf.z = 0u;
          pf.w = 0u;
        }
        o1 = mfma(v4u{b0.x, b0.y, b1.x, b1.y}, pf, o1);
        o0 = mfma(v4u{a0.x, a0.y, a1.x, a1.y}, pf, o0);
        __builtin_amdgcn_sched_barrier(0);
      }
      const float inv = __builtin_amdgcn_rcpf(xrow_sum(g == 3 ? o1[0] : 0.f));
      const v4u of = {pk2(o0[0] * inv, o0[1] * inv), pk2(o0[2] * inv, o0[3] * inv), pk2(o1[0] * inv, o1[1] * inv),
                      pk2(o1[2] * inv, o1[3] * inv)};
      __builtin_amdgcn_raw_buffer_store_b128(of, rs, lane * 16, obase + (w * 16 + h) * 1024, 16);
    }
    STAMP(ws, 16 + layer * 12);
    {
      LANE();
      if (layer) {
        const f32x2 sg = *(const LAS f32x2*)(sm + M_S_G + (rb * 32 + (lane & 31)) * 8);
#pragma unroll
        for (int j = 0; j < 4; ++j) xres[j] = gate_exact(xres[j], yres[j], sg.x, sg.y, sg.x, sg.y);
        asm volatile("" : "+v"(xres));
      }
      __builtin_amdgcn_s_sleep(DLY_O);
      v4u ot[4];
      for (unsigned spins = 0;; ++spins) {
        asm volatile("" ::: "memory");
#pragma unroll
        for (int i = 0; i < 4; ++i) ot[i] = __builtin_amdgcn_raw_buffer_load_b128(rs, lane * 16, obase + rb * 32768 + w * 4096 + i * 1024, 16);
        if (!__any(max(max(smax(ot[0]), smax(ot[1])), max(smax(ot[2]), smax(ot[3]))) == SENT) || spins > SPIN_LIM) break;
      }
#pragma unroll
      for (int i = 0; i < 4; ++i) *(LAS v4u*)(sm + M_OFF_K + w * 4096 + i * 1024 + lane * 16) = ot[i];
      bar();
      if (layer == 0) {
        const unsigned xf_off = (g * S + c) * 16;
        unsigned so = OFF_YF + (ti * 128 + w * 16) * 16;
#pragma unroll
        for (int ks = 0; ks < 14; ++ks) {
          Y[ks] = __builtin_amdgcn_raw_buffer_load_b128(rs, xf_off, so, 0);
          so += 4 * S * 16;
          asm volatile("" : "+s"(so));
        }
      } else {
#pragma unroll
        for (int ks = 0; ks < 14; ++ks) asm volatile("" : "=v"(Y[ks]));
      }
    }
    STAMP(ws, 17 + layer * 12);
    if (w_ < 7) {
      LANE();
      const lptr ol = sm + M_OFF_K + lane * 16;
      f32x4 a0 = {0.f, 0.f, 0.f, 0.f}, a1 = {0.f, 0.f, 0.f, 0.f};
      v4u oa[4][2];
#pragma unroll
      for (int i = 0; i < 3; ++i) {
        oa[i][0] = *(const LAS v4u*)(ol + i * 1024);
        oa[i][1] = *(const LAS v4u*)(ol + 16384 + i * 1024);
      }
#pragma unroll
      for (int ks = 0; ks < 16; ++ks) {
        if (ks + 3 < 16) {
          oa[(ks + 3) & 3][0] = *(const LAS v4u*)(ol + (ks + 3) * 1024);
          oa[(ks + 3) & 3][1] = *(const LAS v4u*)(ol + 16384 + (ks + 3) * 1024);
        }
        a0 = mfma(WO[ks], oa[ks & 3][0], a0);
        a1 = mfma(WO[ks], oa[ks & 3][1], a1);
        __builtin_amdgcn_sched_barrier(0);
      }
      const auto e0 = __builtin_amdgcn_permlane16_swap(pk2(a0[0], a0[1]), pk2(a1[0], a1[1]), false, false);
      const auto e1 = __builtin_amdgcn_permlane16_swap(pk2(a0[2], a0[3]), pk2(a1[2], a1[3]), false, false);
      const unsigned at[4] = {e0[0], e1[0], e0[1], e1[1]};
#pragma unroll
      for (int j = 0; j < 4; ++j) xres[j] = pk2(bflo(xres[j]) + bflo(at[j]), bfhi(xres[j]) + bfhi(at[j]));
      if (layer == 0) {
        const unsigned xo_off = ((g >> 1) * S + (lane & 31)) * 16;
        __builtin_amdgcn_raw_buffer_store_b128(xres, rs, xo_off, OFF_X2F + (ct * 2 * S + ti * 128 + rb * 32) * 16, 16);
        float s = 0.f, q = 0.f;
#pragma unroll
        for (int j = 0; j < 4; ++j) {
          const float d0 = bflo(xres[j]), d1 = bfhi(xres[j]);
          s += d0 + d1;
          q = fmaf(d0, d0, fmaf(d1, d1, q));
        }
        const auto s2 = __builtin_amdgcn_permlane32_swap(__float_as_uint(s), __float_as_uint(q), false, false);
        *(LAS float*)(sm + M_S_P + (w * 32 + (lane & 31)) * 8 + (lane >> 5) * 4) = __uint_as_float(s2[0]) + __uint_as_float(s2[1]);
      } else {
        *(v4u*)(out + (size_t)(ti * 128 + rb * 32 + (lane & 31)) * D + ct * 16 + (g >> 1) * 8) = xres;
      }
    }
    STAMP(ws, 18 + layer * 12);
    if (layer == 0) {
      bar();
      LANE();
      if (w == 7 && lane < 32) {
        f32x2 a = {0.f, 0.f};
#pragma unroll
        for (int i = 0; i < 7; ++i) a += *(const LAS f32x2*)(sm + M_S_P + (i * 32 + lane) * 8);
        __builtin_amdgcn_raw_buffer_store_b64(__builtin_bit_cast(v2u, a), rs, lane * 32, OFF_ST2 + ((ti * 128 + rb * 32) * 4 + cq) * 8, 16);
      }
    }
    STAMP(ws, 19 + layer * 12);
  }
  STAMP_FLUSH(ws, 7, 31);
  asm volatile("mega_code_end:");
}

extern "C" void kernel_launch(void* const* d_in, const int* in_sizes, int n_in, void* d_out, int out_size, void* d_ws,
                              size_t ws_size, hipStream_t stream) {
  unsigned char* ws = (unsigned char*)d_ws;
  (void)hipFuncSetAttribute((const void*)k_conv, hipFuncAttributeMaxDynamicSharedMemorySize, CV_LDS);
  (void)hipFuncSetAttribute((const void*)k_mega, hipFuncAttributeMaxDynamicSharedMemorySize, M_LDS);
  k_prep<<<256, 512, 0, stream>>>((const u16*)d_in[0], (const u16*)d_in[1], (const u16*)d_in[2], (const u16*)d_in[3],
                                  (const u16*)d_in[7], ws);
  k_conv<<<256, 512, CV_LDS, stream>>>((const u16*)d_in[4], (const u16*)d_in[5], (const u16*)d_in[8], ws);
  k_mega<<<256, 512, M_LDS, stream>>>(ws, (u16*)d_out);
}
```

```cpp
#include <hip/hip_runtime.h>
#include <stdint.h>

#define LAYER_ASM \
  "s_load_dwordx16 s[8:23], %[ka], 0\n" \
  "s_load_dwordx4 s[24:27], %[ka], 64\n" \
  "v_mbcnt_lo_u32_b32 v176, -1, 0\n" \
  "s_getpc_b64 s[60:61]\n" \
  "PFA:\n" \
  "s_lshr_b32 s65, %[b], 3\n" \
  "v_mbcnt_hi_u32_b32 v176, -1, v176\n" \
  "s_lshl_b32 s33, s65, 10\n" \
  "s_cmp_eq_u32 %[w], 0\n" \
  "v_lshl_add_u32 v32, v176, 7, s33\n" \
  "s_cselect_b64 exec, 0xff, 0\n" \
  "v_min_u32 v32, HOTEND-PFA, v32\n" \
  "s_and_b32 s64, %[b], 7\n" \
  "global_load_dword v182, v32, s[60:61]\n" \
  "s_mov_b64 exec, -1\n" \
  "v_lshrrev_b32 v32, 3, v176\n" \
  "v_and_b32 v187, 7, v176\n" \
  "v_and_b32 v34, 2, v32\n" \
  "v_xor_b32 v187, v187, v34\n" \
  "v_mul_u32_u24 v32, 0x380, v32\n" \
  "v_lshl_add_u32 v32, v187, 4, v32\n" \
  "s_lshl_b32 s32, s65, 3\n" \
  "s_add_u32 s32, s32, %[w]\n" \
  "s_mul_i32 s32, s32, 0x1c00\n" \
  "s_lshl_b32 s33, s64, 7\n" \
  "s_add_u32 s32, s32, s33\n" \
  "s_and_b32 s33, %[w], 1\n" \
  "s_lshl_b32 s33, s33, 6\n" \
  "s_lshl_b32 s34, %[w], 10\n" \
  "v_add_u32 v32, s32, v32\n" \
  "v_xor_b32 v187, s33, v32\n" \
  "v_xor_b32 v34, 64, v187\n" \
  "v_and_b32 v177, 15, v176\n" \
  "v_lshlrev_b32 v0, 4, v176\n" \
  "v_subrev_u32 v32, 0x1c00, v32\n" \
  "s_and_b32 s32, %[w], 3\n" \
  "s_lshl_b32 s32, s32, 7\n" \
  "s_lshl_b32 s33, s64, 9\n" \
  "s_add_u32 s32, s32, s33\n" \
  "v_lshl_add_u32 v35, v177, 3, s32\n" \
  "v_add_u32 v28, s34, v0\n" \
  "s_mov_b32 s30, 0x1590000\n" \
  "s_mov_b32 s31, 0x20000\n" \
  "s_cmp_eq_u32 %[w], 0\n" \
  "s_cselect_b32 s35, s65, 0\n" \
  "s_cmp_lg_u32 s35, 0\n" \
  "s_cselect_b64 vcc, -1, 0\n" \
  "s_add_u32 m0, s34, 0x1d600\n" \
  "s_cmp_eq_u32 s64, 7\n" \
  "s_waitcnt lgkmcnt(0)\n" \
  "s_cbranch_scc1 SPARE\n" \
  "global_load_lds_dwordx4 v34, s[10:11]\n" \
  "s_add_u32 m0, s34, 0x1f600\n" \
  "s_mov_b64 s[28:29], s[24:25]\n" \
  "global_load_lds_dwordx4 v187, s[12:13]\n" \
  "s_mov_b32 m0, 0x1d200\n" \
  "s_mov_b64 exec, vcc\n" \
  "global_load_lds_dwordx4 v32, s[10:11]\n" \
  "s_mov_b64 exec, -1\n" \
  "global_load_dwordx2 v[184:185], v35, s[14:15]\n" \
  "buffer_load_dword v180, off, s[28:31], 0 sc1\n" \
  "s_barrier\n" \
  "s_lshl_b32 s66, s64, 5\n" \
  "s_add_u32 s66, s66, s65\n" \
  "v_and_b32 v183, 0xfff, v28\n" \
  "s_mul_i32 s32, s64, 24\n" \
  "s_add_u32 s32, s32, s65\n" \
  "s_mul_i32 s33, s32, 0x493\n" \
  "s_lshr_b32 s33, s33, 16\n" \
  "s_mul_i32 s35, s33, 56\n" \
  "s_sub_u32 s32, s32, s35\n" \
  "s_lshl_b32 s35, s32, 3\n" \
  "s_add_u32 s35, s35, %[w]\n" \
  "s_mul_i32 s35, s35, 0xa80\n" \
  "s_add_u32 s58, s33, 1\n" \
  "s_cmp_eq_u32 s33, 2\n" \
  "s_cselect_b32 s58, 0, s58\n" \
  "s_mul_i32 s58, s58, 0x380\n" \
  "s_add_u32 s35, s35, s58\n" \
  "v_add_u32 v32, s35, v0\n" \
  "s_mul_i32 m0, %[w], 0x420\n" \
  "s_cmp_lt_u32 s65, 24\n" \
  "s_cselect_b32 s58, -1, 0\n" \
  "s_cselect_b32 s59, 0xffffff, 0\n" \
  "s_mov_b64 exec, s[58:59]\n" \
  "global_load_lds_dwordx4 v32, s[20:21]\n" \
  "s_mov_b64 exec, -1\n" \
  "s_mul_i32 s20, s33, 0x7000\n" \
  "s_lshr_b32 s35, s32, 2\n" \
  "s_lshl_b32 s35, s35, 11\n" \
  "s_add_u32 s20, s20, s35\n" \
  "s_and_b32 s35, s32, 3\n" \
  "s_lshl_b32 s35, s35, 8\n" \
  "s_add_u32 s20, s20, s35\n" \
  "s_mul_i32 s35, %[w], 0x2a000\n" \
  "s_add_u32 s20, s20, s35\n" \
  "s_add_u32 s20, s20, 0x1d0000\n" \
  "s_cmp_eq_u32 s33, 0\n" \
  "s_cselect_b32 s21, 0x3f803f80, 0\n" \
  "s_lshl_b32 s33, s66, 13\n" \
  "v_add_u32 v32, s33, v28\n" \
  "global_load_dwordx4 v[188:191], v32, s[16:17]\n" \
  "v_cmp_ne_u32 vcc, 0xff0, v183\n" \
  "s_lshl_b32 s33, s66, 2\n" \
  "v_mov_b32 v35, s33\n" \
  "v_cndmask_b32 v34, 0, 16, vcc\n" \
  "v_add_u32 v32, v32, v34\n" \
  "s_lshl_b64 exec, 1, 63\n" \
  "global_load_dwordx2 v[192:193], v32, s[16:17]\n" \
  "s_mov_b64 exec, -1\n" \
  "global_load_dword v181, v35, s[18:19]\n" \
  "s_lshl_b32 s58, s66, 4\n" \
  "v_lshlrev_b32 v32, 8, v0\n" \
  "s_cmp_eq_u32 %[w], 1\n" \
  "s_cselect_b64 exec, 14, 0\n" \
  "buffer_load_dword v213, v32, s[28:31], s58 offen sc1\n" \
  "s_mov_b64 exec, -1\n" \
  "v_lshrrev_b32 v178, 4, v176\n" \
  "v_mov_b32 v212, 0x3f803f80\n" \
  "v_mov_b32 v196, 0\n" \
  "v_mov_b32 v197, 0\n" \
  "v_mov_b32 v198, 0\n" \
  "v_mov_b32 v199, 0\n" \
  "s_lshl_b32 s71, s66, 13\n" \
  "s_add_u32 s71, s71, 0x790000\n" \
  "v_mov_b32 v48, -1\n" \
  "v_mov_b32 v49, -1\n" \
  "v_mov_b32 v50, -1\n" \
  "v_mov_b32 v51, -1\n" \
  "v_bfe_u32 v33, v176, 2, 2\n" \
  "v_and_b32 v35, 3, v176\n" \
  "s_and_b32 s32, %[w], 3\n" \
  "s_lshl_b32 s33, s32, 1\n" \
  "v_lshrrev_b32 v32, 1, v35\n" \
  "v_add_u32 v32, s33, v32\n" \
  "v_and_b32 v34, 2, v33\n" \
  "v_xor_b32 v32, v32, v34\n" \
  "v_and_b32 v34, 1, v178\n" \
  "v_lshlrev_b32 v34, 2, v34\n" \
  "v_xor_b32 v32, v32, v34\n" \
  "v_lshlrev_b32 v32, 4, v32\n" \
  "v_and_b32 v34, 1, v35\n" \
  "v_lshl_add_u32 v32, v34, 3, v32\n" \
  "v_lshl_add_u32 v34, v178, 3, v33\n" \
  "v_lshl_add_u32 v32, v34, 7, v32\n" \
  "s_lshr_b32 s33, %[w], 2\n" \
  "s_lshl_b32 s34, s33, 12\n" \
  "s_add_u32 s34, s34, 0x1d200\n" \
  "v_add_u32 v200, s34, v32\n" \
  "v_xor_b32 v201, 64, v200\n" \
  "v_lshrrev_b32 v32, 1, v177\n" \
  "v_lshlrev_b32 v32, 13, v32\n" \
  "v_and_b32 v34, 1, v177\n" \
  "v_lshl_add_u32 v179, v34, 5, v32\n" \
  "v_lshl_add_u32 v32, v34, 3, v32\n" \
  "v_lshlrev_b32 v32, 1, v32\n" \
  "v_sub_u32 v34, 3, v178\n" \
  "v_lshl_add_u32 v202, v34, 5, v32\n" \
  "v_and_b32 v34, 2, v178\n" \
  "v_add_u32 v34, v34, v178\n" \
  "v_lshl_add_u32 v179, v34, 4, v179\n" \
  "s_lshl_b32 s34, s64, 2\n" \
  "s_add_u32 s34, s34, s32\n" \
  "s_lshl_b32 s35, s65, 1\n" \
  "s_add_u32 s35, s35, s33\n" \
  "s_lshl_b32 s35, s35, 7\n" \
  "s_lshl_b32 s14, s34, 17\n" \
  "s_lshl_b32 s22, s34, 16\n" \
  "s_add_u32 s22, s22, s35\n" \
  "s_add_u32 s22, s22, 0x10000\n" \
  "s_sub_u32 s14, s14, s35\n" \
  "s_add_u32 s14, s14, 0x991f80\n" \
  "s_add_u32 s15, s14, 0x2000\n" \
  "s_or_b32 s32, s65, %[w]\n" \
  "s_cselect_b64 exec, 0, -1\n" \
  "v_add_u32 v32, 0x1d200, v0\n" \
  "ds_write_b128 v32, v[196:199]\n" \
  "s_mov_b64 exec, -1\n" \
  "v_cmp_eq_u32 vcc, 12, v177\n" \
  "v_lshrrev_b32 v32, 2, v177\n" \
  "v_mul_u32_u24 v220, 0x420, v32\n" \
  "v_and_b32 v32, 3, v177\n" \
  "v_lshl_add_u32 v220, v32, 3, v220\n" \
  "v_lshrrev_b32 v32, 1, v178\n" \
  "v_mul_u32_u24 v221, 0x15000, v32\n" \
  "v_mul_u32_u24 v32, 56, v32\n" \
  "v_and_b32 v34, 1, v178\n" \
  "v_lshl_add_u32 v32, v34, 5, v32\n" \
  "s_mul_i32 s32, %[w], 0x70\n" \
  "v_add3_u32 v220, v220, v32, s32\n" \
  "v_lshl_add_u32 v221, v34, 10, v221\n" \
  "v_lshl_add_u32 v221, v177, 4, v221\n" \
  "v_mov_b32 v222, s21\n" \
  "v_cndmask_b32 v222, 0, v222, vcc\n" \
  "s_lshl_b32 s10, s66, 14\n" \
  "s_add_u32 s10, s10, 0x990000\n" \
  "s_add_u32 s11, s10, 0x2000\n" \
  "s_movk_i32 s57, 0x1000\n" \
  "s_lshr_b32 s33, %[w], 2\n" \
  "s_mul_i32 s33, s33, 0x4100\n" \
  "s_add_u32 s33, s33, 0x15020\n" \
  "v_add_u32 v186, s33, v183\n" \
  "v_lshrrev_b32 v32, 5, v176\n" \
  "v_mul_u32_u24 v32, 0x1240, v32\n" \
  "v_and_b32 v34, 0x1f0, v0\n" \
  "v_add3_u32 v32, v32, v34, 0\n" \
  "v_add_u32 v32, 0x26000, v32\n" \
  "s_cmp_eq_u32 %[w], 1\n" \
  "s_cselect_b64 exec, -1, 0\n" \
  "ds_write_b128 v32, v[196:199]\n" \
  "s_mov_b64 exec, -1\n" \
  "v_mul_u32_u24 v32, 0x1040, v176\n" \
  "v_add_u32 v32, 0x15000, v32\n" \
  "s_cmp_eq_u32 %[w], 2\n" \
  "s_cselect_b64 exec, 0xff, 0\n" \
  "ds_write_b128 v32, v[196:199]\n" \
  "ds_write_b64 v32, v[196:197] offset:16\n" \
  "s_mov_b64 exec, -1\n" \
  "s_and_b32 s32, %[w], 1\n" \
  "s_lshr_b32 s33, %[w], 1\n" \
  "s_lshl_b32 s33, s33, 7\n" \
  "s_mul_i32 s34, s32, 0x4100\n" \
  "s_add_u32 s34, s34, s33\n" \
  "s_add_u32 s34, s34, 0x15000\n" \
  "v_and_b32 v32, 3, v177\n" \
  "v_mul_u32_u24 v32, 0x1040, v32\n" \
  "v_and_b32 v34, 12, v177\n" \
  "v_lshl_add_u32 v32, v34, 1, v32\n" \
  "v_lshl_add_u32 v32, v178, 4, v32\n" \
  "v_add_u32 v215, s34, v32\n" \
  "s_lshl_b32 s58, %[w], 10\n" \
  "s_add_u32 m0, s58, 0x21600\n" \
  "s_nop 0\n" \
  "global_load_lds_dwordx4 v187, s[8:9]\n" \
  "s_lshr_b32 s36, s64, 1\n" \
  "s_lshl_b32 s36, s36, 2\n" \
  "s_lshr_b32 s33, s65, 3\n" \
  "s_add_u32 s36, s36, s33\n" \
  "s_lshl_b32 s33, s36, 3\n" \
  "s_add_u32 s33, s33, %[w]\n" \
  "s_mul_i32 s37, s33, 0x3800\n" \
  "s_and_b32 s46, s64, 1\n" \
  "s_lshl_b32 s46, s46, 3\n" \
  "s_and_b32 s58, s65, 7\n" \
  "s_add_u32 s46, s46, s58\n" \
  "s_lshr_b32 s47, s46, 2\n" \
  "s_and_b32 s48, s46, 3\n" \
  "s_min_u32 s49, %[w], 6\n" \
  "s_mul_i32 s58, s48, 7\n" \
  "s_add_u32 s49, s49, s58\n" \
  "s_lshl_b32 s58, s36, 3\n" \
  "s_add_u32 s58, s58, %[w]\n" \
  "s_lshl_b32 s40, s58, 9\n" \
  "s_add_u32 s40, s40, 0x780000\n" \
  "s_lshl_b32 s53, s58, 4\n" \
  "s_add_u32 s53, s53, s46\n" \
  "s_lshl_b32 s53, s53, 10\n" \
  "s_add_u32 s53, s53, 0xd90000\n" \
  "s_lshl_b32 s50, s66, 4\n" \
  "s_add_u32 s50, s50, %[w]\n" \
  "s_lshl_b32 s50, s50, 10\n" \
  "s_add_u32 s50, s50, 0x990000\n" \
  "s_mov_b32 s58, 0xa0000\n" \
  "s_cmp_lt_u32 s65, 8\n" \
  "s_cselect_b32 s58, s58, 0x20000\n" \
  "s_cmp_gt_u32 s36, 0\n" \
  "s_cselect_b32 s58, s58, 0\n" \
  "s_sub_u32 s51, s50, s58\n" \
  "s_mov_b32 s58, 0xc0000\n" \
  "s_cmp_lt_u32 s65, 16\n" \
  "s_cselect_b32 s58, s58, 0x40000\n" \
  "s_cmp_gt_u32 s36, 1\n" \
  "s_cselect_b32 s58, s58, 0\n" \
  "s_sub_u32 s52, s50, s58\n" \
  "s_lshl_b32 s58, s36, 2\n" \
  "s_add_u32 s58, s58, s47\n" \
  "s_lshl_b32 s54, s58, 3\n" \
  "s_add_u32 s54, s54, %[w]\n" \
  "s_lshl_b32 s54, s54, 12\n" \
  "s_add_u32 s54, s54, 0xd90000\n" \
  "s_mul_i32 s42, s58, 0x7000\n" \
  "s_lshl_b32 s38, s49, 9\n" \
  "s_add_u32 s38, s38, s42\n" \
  "s_lshl_b32 s41, s58, 10\n" \
  "s_lshl_b32 s48, s48, 7\n" \
  "s_add_u32 s41, s41, s48\n" \
  "s_add_u32 s41, s41, 0x780000\n" \
  "s_lshl_b32 s58, s49, 5\n" \
  "s_add_u32 s42, s42, s58\n" \
  "s_lshl_b32 s39, s49, 14\n" \
  "s_add_u32 s39, s39, 0x390000\n" \
  "s_lshl_b32 s43, s47, 8\n" \
  "s_add_u32 s55, s38, 0x790000\n" \
  "s_add_u32 s38, s38, 0x5c0000\n" \
  "s_mov_b32 s56, 0\n" \
  "v_add_u32 v26, 0x2000, v0\n" \
  "s_add_u32 s46, s37, 0x790000\n" \
  "s_add_u32 s47, s37, 0x791000\n" \
  "s_add_u32 s48, s37, 0x792000\n" \
  "s_add_u32 s49, s37, 0x793000\n" \
  "v_add_u32 v7, 0xe000, v0\n" \
  "s_lshl_b32 s58, %[w], 10\n" \
  "s_add_u32 s58, s58, 0x1b000\n" \
  "v_add_u32 v3, s58, v0\n" \
  "s_lshr_b32 s58, %[w], 1\n" \
  "s_lshl_b32 s58, s58, 11\n" \
  "s_add_u32 s58, s58, 0x15000\n" \
  "v_add_u32 v4, s58, v0\n" \
  "s_and_b32 s58, %[w], 1\n" \
  "s_lshl_b32 s58, s58, 3\n" \
  "v_add_u32 v5, s58, v4\n" \
  "s_lshl_b32 s58, %[w], 7\n" \
  "s_add_u32 s58, s58, 0x23000\n" \
  "v_lshl_add_u32 v8, v177, 3, s58\n" \
  "v_lshl_add_u32 v9, v178, 5, s58\n" \
  "s_mov_b32 s58, 0x27480\n" \
  "v_lshl_add_u32 v10, v178, 4, s58\n" \
  "v_lshl_add_u32 v11, v177, 2, s58\n" \
  "s_add_u32 s58, s43, 0x23400\n" \
  "v_lshl_add_u32 v24, v177, 3, s58\n" \
  "v_lshrrev_b32 v34, 1, v178\n" \
  "v_and_b32 v35, 1, v178\n" \
  "v_mul_u32_u24 v13, 0x3800, v35\n" \
  "v_lshl_add_u32 v13, v34, 8, v13\n" \
  "v_lshl_add_u32 v13, v177, 4, v13\n" \
  "v_lshlrev_b32 v27, 5, v177\n" \
  "v_lshl_add_u32 v27, v35, 4, v27\n" \
  "v_mul_u32_u24 v12, 0x3800, v34\n" \
  "v_add_u32 v12, v12, v27\n" \
  "v_lshl_add_u32 v1, v34, 7, v27\n" \
  "v_and_b32 v27, 0x180, v27\n" \
  "v_add_u32 v1, v1, v27\n" \
  "v_cmp_eq_u32 s[60:61], 3, v178\n" \
  "v_cmp_eq_u32 s[62:63], 12, v177\n" \
  "v_and_b32 v34, 1, v177\n" \
  "v_lshlrev_b32 v34, 4, v34\n" \
  "v_mov_b32 v35, 0x3f80\n" \
  "v_lshlrev_b32 v35, v34, v35\n" \
  "v_and_b32 v34, 2, v177\n" \
  "v_cmp_eq_u32 vcc, 0, v34\n" \
  "s_nop 1\n" \
  "v_cndmask_b32 v14, 0, v35, vcc\n" \
  "v_cndmask_b32 v15, v35, 0, vcc\n" \
  "v_lshlrev_b32 v27, 2, v178\n" \
  "v_lshrrev_b32 v34, 1, v177\n" \
  "v_sub_u32 v34, v34, v27\n" \
  "v_sub_u32 v27, v177, v27\n" \
  "v_cmp_eq_u32 vcc, 0, v34\n" \
  "s_nop 1\n" \
  "v_cndmask_b32 v16, 0, v35, vcc\n" \
  "v_cmp_eq_u32 vcc, -8, v34\n" \
  "s_nop 1\n" \
  "v_cndmask_b32 v20, 0, v35, vcc\n" \
  "v_cmp_eq_u32 vcc, 1, v34\n" \
  "s_nop 1\n" \
  "v_cndmask_b32 v17, 0, v35, vcc\n" \
  "v_cmp_eq_u32 vcc, -7, v34\n" \
  "s_nop 1\n" \
  "v_cndmask_b32 v21, 0, v35, vcc\n" \
  "v_cmp_eq_u32 vcc, 2, v34\n" \
  "s_nop 1\n" \
  "v_cndmask_b32 v18, 0, v35, vcc\n" \
  "v_cmp_eq_u32 vcc, -6, v34\n" \
  "s_nop 1\n" \
  "v_cndmask_b32 v22, 0, v35, vcc\n" \
  "v_cmp_eq_u32 vcc, 3, v34\n" \
  "s_nop 1\n" \
  "v_cndmask_b32 v19, 0, v35, vcc\n" \
  "v_cmp_eq_u32 vcc, -5, v34\n" \
  "s_nop 1\n" \
  "v_cndmask_b32 v23, 0, v35, vcc\n" \
  "v_cmp_eq_u32 s[44:45], 0, v27\n" \
  "s_setprio 1\n" \
  "s_waitcnt vmcnt(8)\n" \
  "s_waitcnt lgkmcnt(0)\n" \
  "s_barrier\n" \
  "ds_read_b64_tr_b16 v[112:113], v200 offset:512\n" \
  "ds_read_b64_tr_b16 v[114:115], v201 offset:1024\n" \
  "ds_read_b64_tr_b16 v[120:121], v201 offset:1536\n" \
  "ds_read_b64_tr_b16 v[122:123], v200 offset:9216\n" \
  "ds_read_b64_tr_b16 v[128:129], v200 offset:9728\n" \
  "s_waitcnt vmcnt(7)\n" \
  "v_lshlrev_b32 v216, 16, v184\n" \
  "v_and_b32 v217, 0xffff0000, v184\n" \
  "v_lshlrev_b32 v218, 16, v185\n" \
  "v_and_b32 v219, 0xffff0000, v185\n" \
  "s_waitcnt vmcnt(6)\n" \
  "v_add_u32 v36, 0x3779b1, v180\n" \
  "v_or_b32 v36, 0xff800000, v36\n" \
  "s_nop 0\n" \
  "v_readfirstlane_b32 s67, v36\n" \
  "v_mov_b32 v205, v36\n" \
  "v_mov_b32 v207, v36\n" \
  "v_mov_b32 v209, v36\n" \
  "v_mov_b32 v211, v36\n" \
  "s_waitcnt lgkmcnt(4)\n" \
  "v_and_b32 v57, 0xffff0000, v112\n" \
  "v_lshlrev_b32 v58, 16, v113\n" \
  "v_and_b32 v59, 0xffff0000, v113\n" \
  "s_waitcnt lgkmcnt(3)\n" \
  "v_lshlrev_b32 v64, 16, v114\n" \
  "v_and_b32 v65, 0xffff0000, v114\n" \
  "v_lshlrev_b32 v66, 16, v115\n" \
  "v_and_b32 v67, 0xffff0000, v115\n" \
  "v_mul_f32 v80, v57, v219\n" \
  "v_mul_f32 v81, v58, v219\n" \
  "v_mul_f32 v82, v59, v219\n" \
  "v_mul_f32 v83, v64, v219\n" \
  "v_mul_f32 v96, v58, v218\n" \
  "v_mul_f32 v97, v59, v218\n" \
  "v_mul_f32 v98, v64, v218\n" \
  "v_mul_f32 v99, v65, v218\n" \
  "v_cvt_pk_bf16_f32 v80, v80, v96\n" \
  "v_cvt_pk_bf16_f32 v81, v81, v97\n" \
  "v_cvt_pk_bf16_f32 v82, v82, v98\n" \
  "v_cvt_pk_bf16_f32 v83, v83, v99\n" \
  "v_dot2_f32_bf16 v80, v80, v212, 0\n" \
  "v_dot2_f32_bf16 v81, v81, v212, 0\n" \
  "v_dot2_f32_bf16 v82, v82, v212, 0\n" \
  "v_dot2_f32_bf16 v83, v83, v212, 0\n" \
  "v_mul_f32 v96, v59, v217\n" \
  "v_mul_f32 v97, v64, v217\n" \
  "v_mul_f32 v98, v65, v217\n" \
  "v_mul_f32 v99, v66, v217\n" \
  "v_cvt_pk_bf16_f32 v80, v80, v96\n" \
  "v_cvt_pk_bf16_f32 v81, v81, v97\n" \
  "v_cvt_pk_bf16_f32 v82, v82, v98\n" \
  "v_cvt_pk_bf16_f32 v83, v83, v99\n" \
  "v_dot2_f32_bf16 v80, v80, v212, 0\n" \
  "v_dot2_f32_bf16 v81, v81, v212, 0\n" \
  "v_dot2_f32_bf16 v82, v82, v212, 0\n" \
  "v_dot2_f32_bf16 v83, v83, v212, 0\n" \
  "v_mul_f32 v96, v64, v216\n" \
  "v_mul_f32 v97, v65, v216\n" \
  "v_mul_f32 v98, v66, v216\n" \
  "v_mul_f32 v99, v67, v216\n" \
  "v_cvt_pk_bf16_f32 v80, v80, v96\n" \
  "v_cvt_pk_bf16_f32 v81, v81, v97\n" \
  "v_cvt_pk_bf16_f32 v82, v82, v98\n" \
  "v_cvt_pk_bf16_f32 v83, v83, v99\n" \
  "v_dot2_f32_bf16 v80, v80, v212, 0\n" \
  "v_dot2_f32_bf16 v81, v81, v212, 0\n" \
  "v_dot2_f32_bf16 v82, v82, v212, 0\n" \
  "v_dot2_f32_bf16 v83, v83, v212, 0\n" \
  "v_cvt_pk_bf16_f32 v80, v80, 0\n" \
  "v_cvt_pk_bf16_f32 v81, 0, v81\n" \
  "v_cvt_pk_bf16_f32 v82, v82, 0\n" \
  "v_cvt_pk_bf16_f32 v83, 0, v83\n" \
  "s_waitcnt lgkmcnt(1)\n" \
  "v_dot2_f32_bf16 v80, v80, v122, 0\n" \
  "v_dot2_f32_bf16 v81, v81, v122, 0\n" \
  "v_dot2_f32_bf16 v82, v82, v123, 0\n" \
  "v_dot2_f32_bf16 v83, v83, v123, 0\n" \
  "s_nop 0\n" \
  "v_cvt_pk_bf16_f32 v210, v81, v80\n" \
  "s_nop 0\n" \
  "v_cvt_pk_bf16_f32 v208, v83, v82\n" \
  "v_lshlrev_b32 v72, 16, v120\n" \
  "v_and_b32 v73, 0xffff0000, v120\n" \
  "v_lshlrev_b32 v74, 16, v121\n" \
  "v_and_b32 v75, 0xffff0000, v121\n" \
  "v_mul_f32 v88, v65, v219\n" \
  "v_mul_f32 v89, v66, v219\n" \
  "v_mul_f32 v90, v67, v219\n" \
  "v_mul_f32 v91, v72, v219\n" \
  "v_mul_f32 v104, v66, v218\n" \
  "v_mul_f32 v105, v67, v218\n" \
  "v_mul_f32 v106, v72, v218\n" \
  "v_mul_f32 v107, v73, v218\n" \
  "v_cvt_pk_bf16_f32 v88, v88, v104\n" \
  "v_cvt_pk_bf16_f32 v89, v89, v105\n" \
  "v_cvt_pk_bf16_f32 v90, v90, v106\n" \
  "v_cvt_pk_bf16_f32 v91, v91, v107\n" \
  "v_dot2_f32_bf16 v88, v88, v212, 0\n" \
  "v_dot2_f32_bf16 v89, v89, v212, 0\n" \
  "v_dot2_f32_bf16 v90, v90, v212, 0\n" \
  "v_dot2_f32_bf16 v91, v91, v212, 0\n" \
  "v_mul_f32 v104, v67, v217\n" \
  "v_mul_f32 v105, v72, v217\n" \
  "v_mul_f32 v106, v73, v217\n" \
  "v_mul_f32 v107, v74, v217\n" \
  "v_cvt_pk_bf16_f32 v88, v88, v104\n" \
  "v_cvt_pk_bf16_f32 v89, v89, v105\n" \
  "v_cvt_pk_bf16_f32 v90, v90, v106\n" \
  "v_cvt_pk_bf16_f32 v91, v91, v107\n" \
  "v_dot2_f32_bf16 v88, v88, v212, 0\n" \
  "v_dot2_f32_bf16 v89, v89, v212, 0\n" \
  "v_dot2_f32_bf16 v90, v90, v212, 0\n" \
  "v_dot2_f32_bf16 v91, v91, v212, 0\n" \
  "v_mul_f32 v104, v72, v216\n" \
  "v_mul_f32 v105, v73, v216\n" \
  "v_mul_f32 v106, v74, v216\n" \
  "v_mul_f32 v107, v75, v216\n" \
  "v_cvt_pk_bf16_f32 v88, v88, v104\n" \
  "v_cvt_pk_bf16_f32 v89, v89, v105\n" \
  "v_cvt_pk_bf16_f32 v90, v90, v106\n" \
  "v_cvt_pk_bf16_f32 v91, v91, v107\n" \
  "v_dot2_f32_bf16 v88, v88, v212, 0\n" \
  "v_dot2_f32_bf16 v89, v89, v212, 0\n" \
  "v_dot2_f32_bf16 v90, v90, v212, 0\n" \
  "v_dot2_f32_bf16 v91, v91, v212, 0\n" \
  "v_cvt_pk_bf16_f32 v88, v88, 0\n" \
  "v_cvt_pk_bf16_f32 v89, 0, v89\n" \
  "v_cvt_pk_bf16_f32 v90, v90, 0\n" \
  "v_cvt_pk_bf16_f32 v91, 0, v91\n" \
  "s_waitcnt lgkmcnt(0)\n" \
  "v_dot2_f32_bf16 v88, v88, v128, 0\n" \
  "v_dot2_f32_bf16 v89, v89, v128, 0\n" \
  "v_dot2_f32_bf16 v90, v90, v129, 0\n" \
  "v_dot2_f32_bf16 v91, v91, v129, 0\n" \
  "s_nop 0\n" \
  "v_cvt_pk_bf16_f32 v206, v89, v88\n" \
  "s_nop 0\n" \
  "v_cvt_pk_bf16_f32 v204, v91, v90\n" \
  "s_sleep 8\n" \
  "s_waitcnt vmcnt(0)\n" \
  "buffer_store_dwordx4 v[208:211], v202, s[28:31], s15 offen sc1\n" \
  "buffer_store_dwordx4 v[204:207], v202, s[28:31], s14 offen sc1\n" \
  "s_waitcnt lgkmcnt(0)\n" \
  "s_barrier\n" \
  "ds_read_b64_tr_b16 v[132:133], v220\n" \
  "ds_read_b64_tr_b16 v[134:135], v220 offset:4224\n" \
  "ds_read_b64_tr_b16 v[164:165], v200 offset:17408\n" \
  "ds_read_b64_tr_b16 v[166:167], v200 offset:17920\n" \
  "s_mov_b32 s58, 0xf0000000\n" \
  "s_mov_b32 s59, 0xf0000000\n" \
  "s_mov_b64 exec, s[58:59]\n" \
  "s_waitcnt lgkmcnt(3)\n" \
  "v_mov_b32 v132, v222\n" \
  "v_mov_b32 v133, v222\n" \
  "s_waitcnt lgkmcnt(2)\n" \
  "v_mov_b32 v134, v222\n" \
  "v_mov_b32 v135, v222\n" \
  "s_cmp_lt_u32 s65, 24\n" \
  "s_cselect_b64 exec, -1, 0\n" \
  "buffer_store_dwordx4 v[132:135], v221, s[28:31], s20 offen sc1\n" \
  "s_mov_b64 exec, -1\n" \
  "s_waitcnt lgkmcnt(0)\n" \
  "buffer_store_dwordx4 v[164:167], v179, s[28:31], s22 offen sc1\n" \
  "s_setprio 0\n" \
  "v_cmp_ne_u32 vcc, 0xff0, v183\n" \
  "v_mov_b32_dpp v192, v188 wave_shl:1 row_mask:0xf bank_mask:0xf\n" \
  "v_mov_b32_dpp v193, v189 wave_shl:1 row_mask:0xf bank_mask:0xf\n" \
  "v_cndmask_b32 v192, 0, v192, vcc\n" \
  "v_cndmask_b32 v193, 0, v193, vcc\n" \
  "ds_write_b128 v186, v[190:193] offset:12480\n" \
  "v_alignbit_b32 v136, v189, v188, 16\n" \
  "v_alignbit_b32 v137, v190, v189, 16\n" \
  "v_alignbit_b32 v138, v191, v190, 16\n" \
  "v_alignbit_b32 v139, v192, v191, 16\n" \
  "ds_write_b128 v186, v[136:139]\n" \
  "v_alignbit_b32 v144, v190, v189, 16\n" \
  "v_alignbit_b32 v145, v191, v190, 16\n" \
  "v_alignbit_b32 v146, v192, v191, 16\n" \
  "v_alignbit_b32 v147, v193, v192, 16\n" \
  "ds_write_b128 v186, v[144:147] offset:8320\n" \
  "v_mov_b32 v152, v189\n" \
  "v_mov_b32 v153, v190\n" \
  "v_mov_b32 v154, v191\n" \
  "v_mov_b32 v155, v192\n" \
  "ds_write_b128 v186, v[152:155] offset:4160\n" \
  "v_mov_b32 v160, 0\n" \
  "v_mov_b32 v162, 0\n" \
  "v_lshlrev_b32 v161, 16, v188\n" \
  "v_mov_b32 v163, v188\n" \
  "v_lshlrev_b32 v130, 16, v188\n" \
  "v_mov_b32 v131, v136\n" \
  "v_subrev_u32 v34, 8, v186\n" \
  "s_and_b32 s58, %[w], 3\n" \
  "s_cselect_b64 exec, 0, 1\n" \
  "ds_write_b64 v34, v[160:161]\n" \
  "ds_write_b64 v34, v[162:163] offset:4160\n" \
  "ds_write_b64 v34, v[130:131] offset:8320\n" \
  "ds_write_b64 v34, v[188:189] offset:12480\n" \
  "s_mov_b64 exec, -1\n" \
  "s_waitcnt lgkmcnt(0)\n" \
  "s_barrier\n" \
  "ds_read_b64 v[56:57], v215\n" \
  "ds_read_b64 v[58:59], v215 offset:8\n" \
  "ds_read_b64 v[64:65], v215 offset:64\n" \
  "ds_read_b64 v[66:67], v215 offset:72\n" \
  "ds_read_b64 v[72:73], v215 offset:512\n" \
  "ds_read_b64 v[74:75], v215 offset:520\n" \
  "ds_read_b64 v[80:81], v215 offset:576\n" \
  "ds_read_b64 v[82:83], v215 offset:584\n" \
  "ds_read_b64 v[88:89], v215 offset:1024\n" \
  "ds_read_b64 v[90:91], v215 offset:1032\n" \
  "ds_read_b64 v[96:97], v215 offset:1088\n" \
  "ds_read_b64 v[98:99], v215 offset:1096\n" \
  "ds_read_b64 v[104:105], v215 offset:1536\n" \
  "ds_read_b64 v[106:107], v215 offset:1544\n" \
  "ds_read_b64 v[112:113], v215 offset:1600\n" \
  "ds_read_b64 v[114:115], v215 offset:1608\n" \
  "ds_read_b64 v[120:121], v215 offset:2048\n" \
  "ds_read_b64 v[122:123], v215 offset:2056\n" \
  "ds_read_b64 v[128:129], v215 offset:2112\n" \
  "ds_read_b64 v[130:131], v215 offset:2120\n" \
  "ds_read_b64 v[136:137], v215 offset:2560\n" \
  "ds_read_b64 v[138:139], v215 offset:2568\n" \
  "ds_read_b64 v[144:145], v215 offset:2624\n" \
  "ds_read_b64 v[146:147], v215 offset:2632\n" \
  "ds_read_b64 v[152:153], v215 offset:3072\n" \
  "ds_read_b64 v[154:155], v215 offset:3080\n" \
  "ds_read_b64 v[160:161], v215 offset:3136\n" \
  "ds_read_b64 v[162:163], v215 offset:3144\n" \
  "ds_read_b64 v[168:169], v215 offset:3584\n" \
  "ds_read_b64 v[170:171], v215 offset:3592\n" \
  "ds_read_b64 v[172:173], v215 offset:3648\n" \
  "ds_read_b64 v[174:175], v215 offset:3656\n" \
  "v_lshlrev_b32 v39, 16, v181\n" \
  "s_mov_b32 s58, 0xcccccccc\n" \
  "s_mov_b32 s59, 0xcccccccc\n" \
  "s_mov_b64 exec, s[58:59]\n" \
  "v_and_b32 v39, 0xffff0000, v181\n" \
  "s_mov_b64 exec, -1\n" \
  "s_and_b32 s32, %[w], 1\n" \
  "s_lshr_b32 s33, %[w], 1\n" \
  "s_lshl_b32 s33, s33, 7\n" \
  "s_mul_i32 s34, s32, 0x1240\n" \
  "s_add_u32 s34, s34, s33\n" \
  "s_add_u32 s34, s34, 0x251e0\n" \
  "v_lshlrev_b32 v34, 5, v177\n" \
  "v_sub_u32 v52, s34, v34\n" \
  "v_lshl_add_u32 v52, v178, 4, v52\n" \
  "s_lshl_b32 s34, s32, 12\n" \
  "s_lshl_b32 s33, s33, 3\n" \
  "s_add_u32 s34, s34, s33\n" \
  "s_add_u32 s34, s34, 0x15000\n" \
  "s_lshl_b32 s32, s32, 3\n" \
  "v_and_b32 v32, 2, v178\n" \
  "v_lshl_add_u32 v32, v32, 1, v177\n" \
  "v_add_u32 v32, s32, v32\n" \
  "v_and_b32 v32, 15, v32\n" \
  "v_lshl_add_u32 v32, v178, 4, v32\n" \
  "v_lshl_add_u32 v53, v32, 4, s34\n" \
  "v_lshrrev_b32 v40, 2, v176\n" \
  "v_and_b32 v41, 1, v176\n" \
  "v_bfe_u32 v42, v176, 1, 1\n" \
  "s_lshl_b32 s34, %[w], 9\n" \
  "s_sub_u32 s34, 0x25ff0, s34\n" \
  "v_lshlrev_b32 v34, 5, v40\n" \
  "s_waitcnt vmcnt(2)\n" \
  "buffer_load_dwordx4 v[240:243], v28, s[28:31], s10 offen sc1\n" \
  "buffer_load_dwordx4 v[244:247], v28, s[28:31], s11 offen sc1\n" \
  "v_lshl_add_u32 v34, v41, 4, v34\n" \
  "v_sub_u32 v34, s34, v34\n" \
  "v_mul_u32_u24 v47, 0x1240, v42\n" \
  "v_add_u32 v47, v47, v34\n" \
  "v_lshl_add_u32 v32, v176, 2, v40\n" \
  "v_and_b32 v32, 15, v32\n" \
  "v_lshlrev_b32 v54, 4, v32\n" \
  "v_lshl_add_u32 v54, v41, 9, v54\n" \
  "v_lshl_add_u32 v54, v42, 12, v54\n" \
  "s_lshl_b32 s34, %[w], 13\n" \
  "s_add_u32 s34, s34, 0x15000\n" \
  "v_add_u32 v54, s34, v54\n" \
  "v_mul_u32_u24 v38, 0x3800, v40\n" \
  "v_and_b32 v32, 48, v0\n" \
  "v_add_u32 v38, v38, v32\n" \
  "s_mul_i32 s70, %[w], 0x38000\n" \
  "s_lshl_b32 s33, s66, 6\n" \
  "s_add_u32 s70, s70, s33\n" \
  "s_add_u32 s70, s70, 0x790000\n" \
  "s_lshl_b32 s34, %[w], 11\n" \
  "s_add_u32 s34, s34, 0x950000\n" \
  "v_add_u32 v29, s34, v0\n" \
  "v_mov_b32 v30, 0xffff0000\n" \
  "v_mov_b32 v31, 0x10000\n" \
  "v_lshrrev_b32 v32, 1, v28\n" \
  "v_mul_u32_u24 v203, 0x1238, v41\n" \
  "v_add_u32 v203, v203, v32\n" \
  "v_add_u32 v203, 0x25000, v203\n" \
  "s_lshr_b32 s12, %[w], 1\n" \
  "s_mul_i32 s12, s12, 0x7000\n" \
  "s_and_b32 s19, %[w], 1\n" \
  "s_lshl_b32 s19, s19, 10\n" \
  "s_add_u32 s12, s12, s19\n" \
  "s_and_b32 s19, s64, 1\n" \
  "s_lshl_b32 s19, s19, 3\n" \
  "s_and_b32 s58, s65, 7\n" \
  "s_add_u32 s19, s19, s58\n" \
  "s_mul_i32 s19, s19, 0x15000\n" \
  "s_add_u32 s19, s19, s12\n" \
  "s_add_u32 s19, s19, 0x1d0000\n" \
  "s_cmp_lt_u32 %[w], 6\n" \
  "s_cselect_b64 s[68:69], -1, 0\n" \
  "s_min_u32 s32, %[w], 6\n" \
  "s_lshl_b32 s32, s32, 9\n" \
  "v_and_b32 v33, 31, v176\n" \
  "v_lshl_add_u32 v33, v33, 4, s32\n" \
  "s_mov_b32 s13, 0x1000\n" \
  "s_mov_b64 s[32:33], s[26:27]\n" \
  "s_mov_b32 s34, 0x1c0000\n" \
  "s_mov_b32 s35, 0x20000\n" \
  "s_waitcnt vmcnt(2)\n" \
  "s_barrier\n" \
  "s_lshl_b32 s58, s66, 4\n" \
  "s_add_u32 s58, s58, 0x1000\n" \
  "s_cmp_eq_u32 %[w], 7\n" \
  "s_cselect_b64 exec, 1, 0\n" \
  "buffer_store_dword v36, off, s[28:31], s58 sc1\n" \
  "s_mov_b64 exec, -1\n" \
  "C_kv:\n" \
  "s_waitcnt vmcnt(2)\n" \
  "v_xor_b32 v32, s67, v241\n" \
  "v_xor_b32 v34, s67, v243\n" \
  "s_waitcnt vmcnt(1)\n" \
  "v_xor_b32 v35, s67, v245\n" \
  "v_or3_b32 v32, v32, v34, v35\n" \
  "v_xor_b32 v34, s67, v247\n" \
  "v_or_b32 v32, v32, v34\n" \
  "v_cmp_ne_u32 vcc, 0, v32\n" \
  "s_nop 0\n" \
  "s_cbranch_vccnz F_kv\n" \
  "K_kv:\n" \
  "v_mov_b32 v241, v242\n" \
  "v_mov_b32 v242, v244\n" \
  "v_mov_b32 v243, v246\n" \
  "ds_write_b128 v203, v[240:243]\n" \
  "s_setprio 1\n" \
  "s_waitcnt lgkmcnt(0)\n" \
  "s_barrier\n" \
  "ds_read_b128 v[176:179], v52 offset:3584\n" \
  "ds_read_b128 v[180:183], v52 offset:3648\n" \
  "ds_read_b128 v[184:187], v52 offset:3072\n" \
  "ds_read_b128 v[188:191], v52 offset:3136\n" \
  "ds_read_b128 v[192:195], v52 offset:2560\n" \
  "ds_read_b128 v[196:199], v52 offset:2624\n" \
  "ds_read_b128 v[200:203], v52 offset:2048\n" \
  "ds_read_b128 v[204:207], v52 offset:2112\n" \
  "ds_read_b128 v[208:211], v52 offset:1536\n" \
  "ds_read_b128 v[212:215], v52 offset:1600\n" \
  "ds_read_b128 v[216:219], v52 offset:1024\n" \
  "ds_read_b128 v[220:223], v52 offset:1088\n" \
  "ds_read_b128 v[224:227], v52 offset:512\n" \
  "ds_read_b128 v[228:231], v52 offset:576\n" \
  "ds_read_b128 v[232:235], v52\n" \
  "ds_read_b128 v[236:239], v52 offset:64\n" \
  "ds_read_b128 v[44:47], v47\n" \
  "s_cmp_gt_u32 %[w], 5\n" \
  "s_cbranch_scc1 CVY\n" \
  "s_waitcnt lgkmcnt(15)\n" \
  "v_mfma_f32_16x16x32_bf16 v[248:251], v[152:155], v[176:179], 0\n" \
  "v_mfma_f32_16x16x32_bf16 v[252:255], v[168:171], v[176:179], 0\n" \
  "buffer_load_dword v37, v33, s[28:31], s13 offen sc1\n" \
  "s_waitcnt lgkmcnt(15)\n" \
  "v_mfma_f32_16x16x32_bf16 v[248:251], v[160:163], v[180:183], v[248:251]\n" \
  "v_mfma_f32_16x16x32_bf16 v[252:255], v[172:175], v[180:183], v[252:255]\n" \
  "s_waitcnt lgkmcnt(14)\n" \
  "v_mfma_f32_16x16x32_bf16 v[248:251], v[136:139], v[184:187], v[248:251]\n" \
  "v_mfma_f32_16x16x32_bf16 v[252:255], v[152:155], v[184:187], v[252:255]\n" \
  "s_waitcnt lgkmcnt(13)\n" \
  "v_mfma_f32_16x16x32_bf16 v[248:251], v[144:147], v[188:191], v[248:251]\n" \
  "v_mfma_f32_16x16x32_bf16 v[252:255], v[160:163], v[188:191], v[252:255]\n" \
  "s_waitcnt lgkmcnt(12)\n" \
  "v_mfma_f32_16x16x32_bf16 v[248:251], v[120:123], v[192:195], v[248:251]\n" \
  "s_waitcnt lgkmcnt(0)\n" \
  "v_and_b32 v40, 0xffff0000, v47\n" \
  "v_lshlrev_b32 v41, 16, v47\n" \
  "v_mfma_f32_16x16x32_bf16 v[252:255], v[136:139], v[192:195], v[252:255]\n" \
  "v_and_b32 v42, 0xffff0000, v46\n" \
  "v_lshlrev_b32 v43, 16, v46\n" \
  "v_mfma_f32_16x16x32_bf16 v[248:251], v[128:131], v[196:199], v[248:251]\n" \
  "v_and_b32 v46, 0xffff0000, v44\n" \
  "v_lshlrev_b32 v47, 16, v44\n" \
  "v_mfma_f32_16x16x32_bf16 v[252:255], v[144:147], v[196:199], v[252:255]\n" \
  "v_and_b32 v44, 0xffff0000, v45\n" \
  "v_lshlrev_b32 v45, 16, v45\n" \
  "v_mfma_f32_16x16x32_bf16 v[248:251], v[104:107], v[200:203], v[248:251]\n" \
  "v_mul_f32 v40, v40, v39\n" \
  "v_mul_f32 v44, v44, v39\n" \
  "v_mfma_f32_16x16x32_bf16 v[252:255], v[120:123], v[200:203], v[252:255]\n" \
  "v_mul_f32 v41, v41, v39\n" \
  "v_mul_f32 v45, v45, v39\n" \
  "v_mfma_f32_16x16x32_bf16 v[248:251], v[112:115], v[204:207], v[248:251]\n" \
  "v_mul_f32 v42, v42, v39\n" \
  "v_mul_f32 v46, v46, v39\n" \
  "v_mfma_f32_16x16x32_bf16 v[252:255], v[128:131], v[204:207], v[252:255]\n" \
  "v_mul_f32 v43, v43, v39\n" \
  "v_mul_f32 v47, v47, v39\n" \
  "C_f10:\n" \
  "s_waitcnt vmcnt(0)\n" \
  "v_cmp_ne_u32 vcc, s67, v37\n" \
  "s_nop 0\n" \
  "s_cbranch_vccnz F_f10\n" \
  "K_f10:\n" \
  "s_barrier\n" \
  "v_mfma_f32_16x16x32_bf16 v[248:251], v[88:91], v[208:211], v[248:251]\n" \
  "s_lshl_b32 s10, s66, 13\n" \
  "s_add_u32 s10, s10, 0x10000\n" \
  "buffer_load_dwordx4 v[68:71], v28, s[28:31], s10 offen sc1\n" \
  "buffer_load_dwordx4 v[76:79], v29, s[28:31], 0 offen sc1\n" \
  "buffer_load_dwordx4 v[84:87], v29, s[28:31], 0 offen offset:1024 sc1\n" \
  "v_cvt_pk_bf16_f32 v40, 0, v40\n" \
  "v_cvt_pk_bf16_f32 v44, 0, v44\n" \
  "buffer_load_dwordx4 v[92:95], v0, s[28:31], s19 offen sc1\n" \
  "v_mfma_f32_16x16x32_bf16 v[252:255], v[104:107], v[208:211], v[252:255]\n" \
  "v_cvt_pk_bf16_f32 v41, 0, v41\n" \
  "v_cvt_pk_bf16_f32 v45, 0, v45\n" \
  "v_mfma_f32_16x16x32_bf16 v[248:251], v[96:99], v[212:215], v[248:251]\n" \
  "v_cvt_pk_bf16_f32 v42, 0, v42\n" \
  "v_cvt_pk_bf16_f32 v46, 0, v46\n" \
  "v_mfma_f32_16x16x32_bf16 v[252:255], v[112:115], v[212:215], v[252:255]\n" \
  "v_cvt_pk_bf16_f32 v43, 0, v43\n" \
  "v_cvt_pk_bf16_f32 v47, 0, v47\n" \
  "v_mfma_f32_16x16x32_bf16 v[248:251], v[72:75], v[216:219], v[248:251]\n" \
  "buffer_load_dwordx4 v[100:103], v0, s[28:31], s19 offen offset:2048 sc1\n" \
  "s_add_u32 s19, s19, 0x1000\n" \
  "v_mfma_f32_16x16x32_bf16 v[252:255], v[88:91], v[216:219], v[252:255]\n" \
  "v_mfma_f32_16x16x32_bf16 v[248:251], v[80:83], v[220:223], v[248:251]\n" \
  "v_mfma_f32_16x16x32_bf16 v[252:255], v[96:99], v[220:223], v[252:255]\n" \
  "v_mfma_f32_16x16x32_bf16 v[248:251], v[56:59], v[224:227], v[248:251]\n" \
  "buffer_load_dwordx4 v[108:111], v0, s[28:31], s19 offen sc1\n" \
  "v_mfma_f32_16x16x32_bf16 v[252:255], v[72:75], v[224:227], v[252:255]\n" \
  "v_mfma_f32_16x16x32_bf16 v[248:251], v[64:67], v[228:231], v[248:251]\n" \
  "v_mfma_f32_16x16x32_bf16 v[252:255], v[80:83], v[228:231], v[252:255]\n" \
  "v_mfma_f32_16x16x32_bf16 v[252:255], v[56:59], v[232:235], v[252:255]\n" \
  "buffer_load_dwordx4 v[116:119], v0, s[28:31], s19 offen offset:2048 sc1\n" \
  "s_add_u32 s19, s19, 0x1000\n" \
  "v_mfma_f32_16x16x32_bf16 v[252:255], v[64:67], v[236:239], v[252:255]\n" \
  "v_mfma_f32_16x16x32_bf16 v[240:243], v[120:123], v[176:179], 0\n" \
  "v_mfma_f32_16x16x32_bf16 v[244:247], v[136:139], v[176:179], 0\n" \
  "v_mfma_f32_16x16x32_bf16 v[240:243], v[128:131], v[180:183], v[240:243]\n" \
  "buffer_load_dwordx4 v[124:127], v0, s[28:31], s19 offen sc1\n" \
  "v_mfma_f32_16x16x32_bf16 v[244:247], v[144:147], v[180:183], v[244:247]\n" \
  "v_mfma_f32_16x16x32_bf16 v[240:243], v[104:107], v[184:187], v[240:243]\n" \
  "ds_write_b128 v53, v[248:251] offset:49152\n" \
  "s_nop 0\n" \
  "ds_write_b128 v53, v[252:255] offset:57344\n" \
  "v_mfma_f32_16x16x32_bf16 v[244:247], v[120:123], v[184:187], v[244:247]\n" \
  "C_xg0:\n" \
  "s_waitcnt vmcnt(5)\n" \
  "v_max3_u32 v32, v77, v79, v85\n" \
  "v_min3_u32 v34, v77, v79, v85\n" \
  "v_max3_u32 v32, v32, v87, s67\n" \
  "v_min3_u32 v34, v34, v87, s67\n" \
  "v_cmp_ne_u32 vcc, v32, v34\n" \
  "s_nop 0\n" \
  "s_cbranch_vccnz F_xg0\n" \
  "K_xg0:\n" \
  "v_mfma_f32_16x16x32_bf16 v[240:243], v[112:115], v[188:191], v[240:243]\n" \
  "buffer_load_dwordx4 v[132:135], v0, s[28:31], s19 offen offset:2048 sc1\n" \
  "s_add_u32 s19, s19, 0x1000\n" \
  "v_and_b32_dpp v224, v76, v30 quad_perm:[0,1,0,1] row_mask:0xf bank_mask:0xf\n" \
  "v_and_b32_dpp v225, v78, v30 quad_perm:[0,1,0,1] row_mask:0xf bank_mask:0xf\n" \
  "v_mfma_f32_16x16x32_bf16 v[244:247], v[128:131], v[188:191], v[244:247]\n" \
  "v_and_b32_dpp v226, v84, v30 quad_perm:[0,1,0,1] row_mask:0xf bank_mask:0xf\n" \
  "v_and_b32_dpp v227, v86, v30 quad_perm:[0,1,0,1] row_mask:0xf bank_mask:0xf\n" \
  "v_mfma_f32_16x16x32_bf16 v[240:243], v[88:91], v[192:195], v[240:243]\n" \
  "v_and_b32_dpp v228, v76, v30 quad_perm:[2,3,2,3] row_mask:0xf bank_mask:0xf\n" \
  "v_and_b32_dpp v229, v78, v30 quad_perm:[2,3,2,3] row_mask:0xf bank_mask:0xf\n" \
  "v_mfma_f32_16x16x32_bf16 v[244:247], v[104:107], v[192:195], v[244:247]\n" \
  "v_and_b32_dpp v230, v84, v30 quad_perm:[2,3,2,3] row_mask:0xf bank_mask:0xf\n" \
  "v_and_b32_dpp v231, v86, v30 quad_perm:[2,3,2,3] row_mask:0xf bank_mask:0xf\n" \
  "v_mfma_f32_16x16x32_bf16 v[240:243], v[96:99], v[196:199], v[240:243]\n" \
  "buffer_load_dwordx4 v[140:143], v0, s[28:31], s19 offen sc1\n" \
  "v_rcp_f32 v224, v224\n" \
  "v_rcp_f32 v225, v225\n" \
  "v_mfma_f32_16x16x32_bf16 v[244:247], v[112:115], v[196:199], v[244:247]\n" \
  "v_rcp_f32 v226, v226\n" \
  "v_rcp_f32 v227, v227\n" \
  "v_mfma_f32_16x16x32_bf16 v[240:243], v[72:75], v[200:203], v[240:243]\n" \
  "v_rcp_f32 v228, v228\n" \
  "v_rcp_f32 v229, v229\n" \
  "v_mfma_f32_16x16x32_bf16 v[244:247], v[88:91], v[200:203], v[244:247]\n" \
  "v_rcp_f32 v230, v230\n" \
  "v_rcp_f32 v231, v231\n" \
  "v_mfma_f32_16x16x32_bf16 v[240:243], v[80:83], v[204:207], v[240:243]\n" \
  "buffer_load_dwordx4 v[148:151], v0, s[28:31], s19 offen offset:2048 sc1\n" \
  "s_add_u32 s19, s19, 0x1000\n" \
  "v_lshlrev_b32 v152, 16, v68\n" \
  "v_and_b32 v153, 0xffff0000, v68\n" \
  "v_mfma_f32_16x16x32_bf16 v[244:247], v[96:99], v[204:207], v[244:247]\n" \
  "v_lshlrev_b32 v154, 16, v69\n" \
  "v_and_b32 v155, 0xffff0000, v69\n" \
  "v_mfma_f32_16x16x32_bf16 v[240:243], v[56:59], v[208:211], v[240:243]\n" \
  "v_lshlrev_b32 v160, 16, v70\n" \
  "v_and_b32 v161, 0xffff0000, v70\n" \
  "v_mfma_f32_16x16x32_bf16 v[244:247], v[72:75], v[208:211], v[244:247]\n" \
  "v_lshlrev_b32 v162, 16, v71\n" \
  "v_and_b32 v163, 0xffff0000, v71\n" \
  "v_mfma_f32_16x16x32_bf16 v[240:243], v[64:67], v[212:215], v[240:243]\n" \
  "buffer_load_dwordx4 v[156:159], v0, s[28:31], s19 offen sc1\n" \
  "v_mul_u32_u24_dpp v168, v76, v31 quad_perm:[0,1,0,1] row_mask:0xf bank_mask:0xf\n" \
  "v_mul_u32_u24_dpp v169, v78, v31 quad_perm:[0,1,0,1] row_mask:0xf bank_mask:0xf\n" \
  "v_mfma_f32_16x16x32_bf16 v[244:247], v[80:83], v[212:215], v[244:247]\n" \
  "v_mul_u32_u24_dpp v170, v84, v31 quad_perm:[0,1,0,1] row_mask:0xf bank_mask:0xf\n" \
  "v_mul_u32_u24_dpp v171, v86, v31 quad_perm:[0,1,0,1] row_mask:0xf bank_mask:0xf\n" \
  "v_mfma_f32_16x16x32_bf16 v[244:247], v[56:59], v[216:219], v[244:247]\n" \
  "v_mul_u32_u24_dpp v172, v76, v31 quad_perm:[2,3,2,3] row_mask:0xf bank_mask:0xf\n" \
  "v_mul_u32_u24_dpp v173, v78, v31 quad_perm:[2,3,2,3] row_mask:0xf bank_mask:0xf\n" \
  "v_mfma_f32_16x16x32_bf16 v[244:247], v[64:67], v[220:223], v[244:247]\n" \
  "v_mul_u32_u24_dpp v174, v84, v31 quad_perm:[2,3,2,3] row_mask:0xf bank_mask:0xf\n" \
  "v_mul_u32_u24_dpp v175, v86, v31 quad_perm:[2,3,2,3] row_mask:0xf bank_mask:0xf\n" \
  "v_mfma_f32_16x16x32_bf16 v[248:251], v[88:91], v[176:179], 0\n" \
  "buffer_load_dwordx4 v[164:167], v0, s[28:31], s19 offen offset:2048 sc1\n" \
  "s_add_u32 s19, s19, 0x1000\n" \
  "v_sub_f32 v168, v152, v168\n" \
  "v_sub_f32 v169, v153, v169\n" \
  "v_mfma_f32_16x16x32_bf16 v[252:255], v[104:107], v[176:179], 0\n" \
  "v_sub_f32 v170, v154, v170\n" \
  "v_sub_f32 v171, v155, v171\n" \
  "v_mfma_f32_16x16x32_bf16 v[248:251], v[96:99], v[180:183], v[248:251]\n" \
  "v_sub_f32 v172, v160, v172\n" \
  "v_sub_f32 v173, v161, v173\n" \
  "v_mfma_f32_16x16x32_bf16 v[252:255], v[112:115], v[180:183], v[252:255]\n" \
  "v_sub_f32 v174, v162, v174\n" \
  "v_sub_f32 v175, v163, v175\n" \
  "v_mfma_f32_16x16x32_bf16 v[248:251], v[72:75], v[184:187], v[248:251]\n" \
  "ds_write_b128 v53, v[240:243] offset:32768\n" \
  "ds_write_b128 v53, v[244:247] offset:40960\n" \
  "buffer_load_dwordx4 v[60:63], v0, s[28:31], s19 offen sc1\n" \
  "v_cvt_pk_bf16_f32 v168, 0, v168\n" \
  "v_cvt_pk_bf16_f32 v169, 0, v169\n" \
  "v_mfma_f32_16x16x32_bf16 v[252:255], v[88:91], v[184:187], v[252:255]\n" \
  "v_cvt_pk_bf16_f32 v170, 0, v170\n" \
  "v_cvt_pk_bf16_f32 v171, 0, v171\n" \
  "v_mfma_f32_16x16x32_bf16 v[248:251], v[80:83], v[188:191], v[248:251]\n" \
  "v_cvt_pk_bf16_f32 v172, 0, v172\n" \
  "v_cvt_pk_bf16_f32 v173, 0, v173\n" \
  "v_mfma_f32_16x16x32_bf16 v[252:255], v[96:99], v[188:191], v[252:255]\n" \
  "v_cvt_pk_bf16_f32 v174, 0, v174\n" \
  "v_cvt_pk_bf16_f32 v175, 0, v175\n" \
  "v_mfma_f32_16x16x32_bf16 v[248:251], v[56:59], v[192:195], v[248:251]\n" \
  "buffer_load_dwordx4 v[232:235], v0, s[28:31], s19 offen offset:2048 sc1\n" \
  "s_add_u32 s19, s19, 0x1000\n" \
  "v_mul_f32 v168, v168, v224\n" \
  "v_mul_f32 v169, v169, v225\n" \
  "v_mfma_f32_16x16x32_bf16 v[252:255], v[72:75], v[192:195], v[252:255]\n" \
  "v_mul_f32 v170, v170, v226\n" \
  "v_mul_f32 v171, v171, v227\n" \
  "v_mfma_f32_16x16x32_bf16 v[248:251], v[64:67], v[196:199], v[248:251]\n" \
  "v_mul_f32 v172, v172, v228\n" \
  "v_mul_f32 v173, v173, v229\n" \
  "v_mfma_f32_16x16x32_bf16 v[252:255], v[80:83], v[196:199], v[252:255]\n" \
  "v_mul_f32 v174, v174, v230\n" \
  "v_mul_f32 v175, v175, v231\n" \
  "v_mfma_f32_16x16x32_bf16 v[252:255], v[56:59], v[200:203], v[252:255]\n" \
  "buffer_load_dwordx4 v[236:239], v0, s[28:31], s19 offen sc1\n" \
  "v_cvt_pk_bf16_f32 v168, v168, 0\n" \
  "v_cvt_pk_bf16_f32 v169, 0, v169\n" \
  "v_mfma_f32_16x16x32_bf16 v[252:255], v[64:67], v[204:207], v[252:255]\n" \
  "v_cvt_pk_bf16_f32 v170, v170, 0\n" \
  "v_cvt_pk_bf16_f32 v171, 0, v171\n" \
  "v_mfma_f32_16x16x32_bf16 v[240:243], v[56:59], v[176:179], 0\n" \
  "v_cvt_pk_bf16_f32 v172, v172, 0\n" \
  "v_cvt_pk_bf16_f32 v173, 0, v173\n" \
  "v_mfma_f32_16x16x32_bf16 v[244:247], v[72:75], v[176:179], 0\n" \
  "v_cvt_pk_bf16_f32 v174, v174, 0\n" \
  "v_cvt_pk_bf16_f32 v175, 0, v175\n" \
  "v_mfma_f32_16x16x32_bf16 v[240:243], v[64:67], v[180:183], v[240:243]\n" \
  "buffer_load_dwordx4 v[136:139], v0, s[28:31], s19 offen offset:2048 sc1\n" \
  "v_mfma_f32_16x16x32_bf16 v[244:247], v[80:83], v[180:183], v[244:247]\n" \
  "v_mfma_f32_16x16x32_bf16 v[244:247], v[56:59], v[184:187], v[244:247]\n" \
  "ds_write_b128 v53, v[248:251] offset:16384\n" \
  "ds_write_b128 v53, v[252:255] offset:24576\n" \
  "v_mfma_f32_16x16x32_bf16 v[244:247], v[64:67], v[188:191], v[244:247]\n" \
  "s_nop 1\n" \
  "ds_write_b128 v53, v[240:243]\n" \
  "s_nop 4\n" \
  "ds_write_b128 v53, v[244:247] offset:8192\n" \
  "s_branch CVJ\n" \
  "CVY:\n" \
  "s_waitcnt lgkmcnt(15)\n" \
  "v_mfma_f32_16x16x32_bf16 v[248:251], v[152:155], v[176:179], 0\n" \
  "s_cmp_eq_u32 %[w], 7\n" \
  "s_cselect_b64 exec, -1, 0\n" \
  "buffer_store_dwordx4 v[48:51], v0, s[28:31], s71 offen sc1\n" \
  "buffer_store_dwordx4 v[48:51], v0, s[28:31], s71 offen offset:1024 sc1\n" \
  "buffer_store_dwordx4 v[48:51], v0, s[28:31], s71 offen offset:2048 sc1\n" \
  "buffer_store_dwordx4 v[48:51], v0, s[28:31], s71 offen offset:3072 sc1\n" \
  "s_add_u32 s71, s71, 0x1000\n" \
  "buffer_store_dwordx4 v[48:51], v0, s[28:31], s71 offen sc1\n" \
  "buffer_store_dwordx4 v[48:51], v0, s[28:31], s71 offen offset:1024 sc1\n" \
  "buffer_store_dwordx4 v[48:51], v0, s[28:31], s71 offen offset:2048 sc1\n" \
  "buffer_store_dwordx4 v[48:51], v0, s[28:31], s71 offen offset:3072 sc1\n" \
  "s_mov_b64 exec, -1\n" \
  "s_mov_b32 s19, 0x2000\n" \
  "v_mfma_f32_16x16x32_bf16 v[252:255], v[168:171], v[176:179], 0\n" \
  "buffer_load_dword v37, v33, s[28:31], s13 offen sc1\n" \
  "s_waitcnt lgkmcnt(15)\n" \
  "v_mfma_f32_16x16x32_bf16 v[248:251], v[160:163], v[180:183], v[248:251]\n" \
  "v_mfma_f32_16x16x32_bf16 v[252:255], v[172:175], v[180:183], v[252:255]\n" \
  "s_waitcnt lgkmcnt(14)\n" \
  "v_mfma_f32_16x16x32_bf16 v[248:251], v[136:139], v[184:187], v[248:251]\n" \
  "v_mfma_f32_16x16x32_bf16 v[252:255], v[152:155], v[184:187], v[252:255]\n" \
  "s_waitcnt lgkmcnt(13)\n" \
  "v_mfma_f32_16x16x32_bf16 v[248:251], v[144:147], v[188:191], v[248:251]\n" \
  "v_mfma_f32_16x16x32_bf16 v[252:255], v[160:163], v[188:191], v[252:255]\n" \
  "s_waitcnt lgkmcnt(12)\n" \
  "v_mfma_f32_16x16x32_bf16 v[248:251], v[120:123], v[192:195], v[248:251]\n" \
  "s_waitcnt lgkmcnt(0)\n" \
  "v_and_b32 v40, 0xffff0000, v47\n" \
  "v_lshlrev_b32 v41, 16, v47\n" \
  "v_mfma_f32_16x16x32_bf16 v[252:255], v[136:139], v[192:195], v[252:255]\n" \
  "v_and_b32 v42, 0xffff0000, v46\n" \
  "v_lshlrev_b32 v43, 16, v46\n" \
  "v_mfma_f32_16x16x32_bf16 v[248:251], v[128:131], v[196:199], v[248:251]\n" \
  "v_and_b32 v46, 0xffff0000, v44\n" \
  "v_lshlrev_b32 v47, 16, v44\n" \
  "v_mfma_f32_16x16x32_bf16 v[252:255], v[144:147], v[196:199], v[252:255]\n" \
  "v_and_b32 v44, 0xffff0000, v45\n" \
  "v_lshlrev_b32 v45, 16, v45\n" \
  "v_mfma_f32_16x16x32_bf16 v[248:251], v[104:107], v[200:203], v[248:251]\n" \
  "v_mul_f32 v40, v40, v39\n" \
  "v_mul_f32 v44, v44, v39\n" \
  "v_mfma_f32_16x16x32_bf16 v[252:255], v[120:123], v[200:203], v[252:255]\n" \
  "v_mul_f32 v41, v41, v39\n" \
  "v_mul_f32 v45, v45, v39\n" \
  "v_mfma_f32_16x16x32_bf16 v[248:251], v[112:115], v[204:207], v[248:251]\n" \
  "v_mul_f32 v42, v42, v39\n" \
  "v_mul_f32 v46, v46, v39\n" \
  "v_mfma_f32_16x16x32_bf16 v[252:255], v[128:131], v[204:207], v[252:255]\n" \
  "v_mul_f32 v43, v43, v39\n" \
  "v_mul_f32 v47, v47, v39\n" \
  "s_waitcnt vmcnt(1)\n" \
  "s_lshl_b32 s58, s66, 4\n" \
  "s_add_u32 s58, s58, 0x2000\n" \
  "s_cmp_eq_u32 %[w], 7\n" \
  "s_cselect_b64 exec, 1, 0\n" \
  "buffer_store_dword v36, off, s[28:31], s58 sc1\n" \
  "s_mov_b64 exec, -1\n" \
  "C_f11:\n" \
  "s_waitcnt vmcnt(1)\n" \
  "v_cmp_ne_u32 vcc, s67, v37\n" \
  "s_nop 0\n" \
  "s_cbranch_vccnz F_f11\n" \
  "K_f11:\n" \
  "s_barrier\n" \
  "v_mfma_f32_16x16x32_bf16 v[248:251], v[88:91], v[208:211], v[248:251]\n" \
  "s_lshl_b32 s10, s66, 13\n" \
  "s_add_u32 s10, s10, 0x10000\n" \
  "buffer_load_dwordx4 v[68:71], v28, s[28:31], s10 offen sc1\n" \
  "buffer_load_dwordx4 v[76:79], v29, s[28:31], 0 offen sc1\n" \
  "buffer_load_dwordx4 v[84:87], v29, s[28:31], 0 offen offset:1024 sc1\n" \
  "v_cvt_pk_bf16_f32 v40, 0, v40\n" \
  "v_cvt_pk_bf16_f32 v44, 0, v44\n" \
  "v_mfma_f32_16x16x32_bf16 v[252:255], v[104:107], v[208:211], v[252:255]\n" \
  "v_cvt_pk_bf16_f32 v41, 0, v41\n" \
  "v_cvt_pk_bf16_f32 v45, 0, v45\n" \
  "v_mfma_f32_16x16x32_bf16 v[248:251], v[96:99], v[212:215], v[248:251]\n" \
  "v_cvt_pk_bf16_f32 v42, 0, v42\n" \
  "v_cvt_pk_bf16_f32 v46, 0, v46\n" \
  "v_mfma_f32_16x16x32_bf16 v[252:255], v[112:115], v[212:215], v[252:255]\n" \
  "v_cvt_pk_bf16_f32 v43, 0, v43\n" \
  "v_cvt_pk_bf16_f32 v47, 0, v47\n" \
  "v_mfma_f32_16x16x32_bf16 v[248:251], v[72:75], v[216:219], v[248:251]\n" \
  "v_mfma_f32_16x16x32_bf16 v[252:255], v[88:91], v[216:219], v[252:255]\n" \
  "v_mfma_f32_16x16x32_bf16 v[248:251], v[80:83], v[220:223], v[248:251]\n" \
  "v_mfma_f32_16x16x32_bf16 v[252:255], v[96:99], v[220:223], v[252:255]\n" \
  "v_mfma_f32_16x16x32_bf16 v[248:251], v[56:59], v[224:227], v[248:251]\n" \
  "v_mfma_f32_16x16x32_bf16 v[252:255], v[72:75], v[224:227], v[252:255]\n" \
  "v_mfma_f32_16x16x32_bf16 v[248:251], v[64:67], v[228:231], v[248:251]\n" \
  "v_mfma_f32_16x16x32_bf16 v[252:255], v[80:83], v[228:231], v[252:255]\n" \
  "v_mfma_f32_16x16x32_bf16 v[252:255], v[56:59], v[232:235], v[252:255]\n" \
  "v_mfma_f32_16x16x32_bf16 v[252:255], v[64:67], v[236:239], v[252:255]\n" \
  "v_mfma_f32_16x16x32_bf16 v[240:243], v[120:123], v[176:179], 0\n" \
  "v_mfma_f32_16x16x32_bf16 v[244:247], v[136:139], v[176:179], 0\n" \
  "v_mfma_f32_16x16x32_bf16 v[240:243], v[128:131], v[180:183], v[240:243]\n" \
  "v_mfma_f32_16x16x32_bf16 v[244:247], v[144:147], v[180:183], v[244:247]\n" \
  "v_mfma_f32_16x16x32_bf16 v[240:243], v[104:107], v[184:187], v[240:243]\n" \
  "ds_write_b128 v53, v[248:251] offset:49152\n" \
  "s_nop 1\n" \
  "ds_write_b128 v53, v[252:255] offset:57344\n" \
  "v_mfma_f32_16x16x32_bf16 v[244:247], v[120:123], v[184:187], v[244:247]\n" \
  "C_xg1:\n" \
  "s_waitcnt vmcnt(0)\n" \
  "v_max3_u32 v32, v77, v79, v85\n" \
  "v_min3_u32 v34, v77, v79, v85\n" \
  "v_max3_u32 v32, v32, v87, s67\n" \
  "v_min3_u32 v34, v34, v87, s67\n" \
  "v_cmp_ne_u32 vcc, v32, v34\n" \
  "s_nop 0\n" \
  "s_cbranch_vccnz F_xg1\n" \
  "K_xg1:\n" \
  "v_mfma_f32_16x16x32_bf16 v[240:243], v[112:115], v[188:191], v[240:243]\n" \
  "v_and_b32_dpp v224, v76, v30 quad_perm:[0,1,0,1] row_mask:0xf bank_mask:0xf\n" \
  "v_and_b32_dpp v225, v78, v30 quad_perm:[0,1,0,1] row_mask:0xf bank_mask:0xf\n" \
  "v_mfma_f32_16x16x32_bf16 v[244:247], v[128:131], v[188:191], v[244:247]\n" \
  "v_and_b32_dpp v226, v84, v30 quad_perm:[0,1,0,1] row_mask:0xf bank_mask:0xf\n" \
  "v_and_b32_dpp v227, v86, v30 quad_perm:[0,1,0,1] row_mask:0xf bank_mask:0xf\n" \
  "v_mfma_f32_16x16x32_bf16 v[240:243], v[88:91], v[192:195], v[240:243]\n" \
  "v_and_b32_dpp v228, v76, v30 quad_perm:[2,3,2,3] row_mask:0xf bank_mask:0xf\n" \
  "v_and_b32_dpp v229, v78, v30 quad_perm:[2,3,2,3] row_mask:0xf bank_mask:0xf\n" \
  "v_mfma_f32_16x16x32_bf16 v[244:247], v[104:107], v[192:195], v[244:247]\n" \
  "v_and_b32_dpp v230, v84, v30 quad_perm:[2,3,2,3] row_mask:0xf bank_mask:0xf\n" \
  "v_and_b32_dpp v231, v86, v30 quad_perm:[2,3,2,3] row_mask:0xf bank_mask:0xf\n" \
  "v_mfma_f32_16x16x32_bf16 v[240:243], v[96:99], v[196:199], v[240:243]\n" \
  "v_rcp_f32 v224, v224\n" \
  "v_rcp_f32 v225, v225\n" \
  "v_mfma_f32_16x16x32_bf16 v[244:247], v[112:115], v[196:199], v[244:247]\n" \
  "v_rcp_f32 v226, v226\n" \
  "v_rcp_f32 v227, v227\n" \
  "v_mfma_f32_16x16x32_bf16 v[240:243], v[72:75], v[200:203], v[240:243]\n" \
  "v_rcp_f32 v228, v228\n" \
  "v_rcp_f32 v229, v229\n" \
  "v_mfma_f32_16x16x32_bf16 v[244:247], v[88:91], v[200:203], v[244:247]\n" \
  "v_rcp_f32 v230, v230\n" \
  "v_rcp_f32 v231, v231\n" \
  "v_mfma_f32_16x16x32_bf16 v[240:243], v[80:83], v[204:207], v[240:243]\n" \
  "v_lshlrev_b32 v152, 16, v68\n" \
  "v_and_b32 v153, 0xffff0000, v68\n" \
  "v_mfma_f32_16x16x32_bf16 v[244:247], v[96:99], v[204:207], v[244:247]\n" \
  "v_lshlrev_b32 v154, 16, v69\n" \
  "v_and_b32 v155, 0xffff0000, v69\n" \
  "v_mfma_f32_16x16x32_bf16 v[240:243], v[56:59], v[208:211], v[240:243]\n" \
  "v_lshlrev_b32 v160, 16, v70\n" \
  "v_and_b32 v161, 0xffff0000, v70\n" \
  "v_mfma_f32_16x16x32_bf16 v[244:247], v[72:75], v[208:211], v[244:247]\n" \
  "v_lshlrev_b32 v162, 16, v71\n" \
  "v_and_b32 v163, 0xffff0000, v71\n" \
  "v_mfma_f32_16x16x32_bf16 v[240:243], v[64:67], v[212:215], v[240:243]\n" \
  "v_mul_u32_u24_dpp v168, v76, v31 quad_perm:[0,1,0,1] row_mask:0xf bank_mask:0xf\n" \
  "v_mul_u32_u24_dpp v169, v78, v31 quad_perm:[0,1,0,1] row_mask:0xf bank_mask:0xf\n" \
  "v_mfma_f32_16x16x32_bf16 v[244:247], v[80:83], v[212:215], v[244:247]\n" \
  "v_mul_u32_u24_dpp v170, v84, v31 quad_perm:[0,1,0,1] row_mask:0xf bank_mask:0xf\n" \
  "v_mul_u32_u24_dpp v171, v86, v31 quad_perm:[0,1,0,1] row_mask:0xf bank_mask:0xf\n" \
  "v_mfma_f32_16x16x32_bf16 v[244:247], v[56:59], v[216:219], v[244:247]\n" \
  "v_mul_u32_u24_dpp v172, v76, v31 quad_perm:[2,3,2,3] row_mask:0xf bank_mask:0xf\n" \
  "v_mul_u32_u24_dpp v173, v78, v31 quad_perm:[2,3,2,3] row_mask:0xf bank_mask:0xf\n" \
  "v_mfma_f32_16x16x32_bf16 v[244:247], v[64:67], v[220:223], v[244:247]\n" \
  "v_mul_u32_u24_dpp v174, v84, v31 quad_perm:[2,3,2,3] row_mask:0xf bank_mask:0xf\n" \
  "v_mul_u32_u24_dpp v175, v86, v31 quad_perm:[2,3,2,3] row_mask:0xf bank_mask:0xf\n" \
  "v_mfma_f32_16x16x32_bf16 v[248:251], v[88:91], v[176:179], 0\n" \
  "buffer_load_dword v92, v0, s[28:31], s19 offen sc1\n" \
  "buffer_load_dword v93, v0, s[28:31], s19 offen offset:1024 sc1\n" \
  "buffer_load_dword v94, v0, s[28:31], s19 offen offset:2048 sc1\n" \
  "buffer_load_dword v95, v0, s[28:31], s19 offen offset:3072 sc1\n" \
  "v_sub_f32 v168, v152, v168\n" \
  "v_sub_f32 v169, v153, v169\n" \
  "v_mfma_f32_16x16x32_bf16 v[252:255], v[104:107], v[176:179], 0\n" \
  "v_sub_f32 v170, v154, v170\n" \
  "v_sub_f32 v171, v155, v171\n" \
  "v_mfma_f32_16x16x32_bf16 v[248:251], v[96:99], v[180:183], v[248:251]\n" \
  "v_sub_f32 v172, v160, v172\n" \
  "v_sub_f32 v173, v161, v173\n" \
  "v_mfma_f32_16x16x32_bf16 v[252:255], v[112:115], v[180:183], v[252:255]\n" \
  "v_sub_f32 v174, v162, v174\n" \
  "v_sub_f32 v175, v163, v175\n" \
  "v_mfma_f32_16x16x32_bf16 v[248:251], v[72:75], v[184:187], v[248:251]\n" \
  "ds_write_b128 v53, v[240:243] offset:32768\n" \
  "ds_write_b128 v53, v[244:247] offset:40960\n" \
  "v_cvt_pk_bf16_f32 v168, 0, v168\n" \
  "v_cvt_pk_bf16_f32 v169, 0, v169\n" \
  "v_mfma_f32_16x16x32_bf16 v[252:255], v[88:91], v[184:187], v[252:255]\n" \
  "v_cvt_pk_bf16_f32 v170, 0, v170\n" \
  "v_cvt_pk_bf16_f32 v171, 0, v171\n" \
  "v_mfma_f32_16x16x32_bf16 v[248:251], v[80:83], v[188:191], v[248:251]\n" \
  "v_cvt_pk_bf16_f32 v172, 0, v172\n" \
  "v_cvt_pk_bf16_f32 v173, 0, v173\n" \
  "v_mfma_f32_16x16x32_bf16 v[252:255], v[96:99], v[188:191], v[252:255]\n" \
  "v_cvt_pk_bf16_f32 v174, 0, v174\n" \
  "v_cvt_pk_bf16_f32 v175, 0, v175\n" \
  "v_mfma_f32_16x16x32_bf16 v[248:251], v[56:59], v[192:195], v[248:251]\n" \
  "v_mul_f32 v168, v168, v224\n" \
  "v_mul_f32 v169, v169, v225\n" \
  "v_mfma_f32_16x16x32_bf16 v[252:255], v[72:75], v[192:195], v[252:255]\n" \
  "v_mul_f32 v170, v170, v226\n" \
  "v_mul_f32 v171, v171, v227\n" \
  "v_mfma_f32_16x16x32_bf16 v[248:251], v[64:67], v[196:199], v[248:251]\n" \
  "v_mul_f32 v172, v172, v228\n" \
  "v_mul_f32 v173, v173, v229\n" \
  "v_mfma_f32_16x16x32_bf16 v[252:255], v[80:83], v[196:199], v[252:255]\n" \
  "v_mul_f32 v174, v174, v230\n" \
  "v_mul_f32 v175, v175, v231\n" \
  "v_mfma_f32_16x16x32_bf16 v[252:255], v[56:59], v[200:203], v[252:255]\n" \
  "v_cvt_pk_bf16_f32 v168, v168, 0\n" \
  "v_cvt_pk_bf16_f32 v169, 0, v169\n" \
  "v_mfma_f32_16x16x32_bf16 v[252:255], v[64:67], v[204:207], v[252:255]\n" \
  "v_cvt_pk_bf16_f32 v170, v170, 0\n" \
  "v_cvt_pk_bf16_f32 v171, 0, v171\n" \
  "v_mfma_f32_16x16x32_bf16 v[240:243], v[56:59], v[176:179], 0\n" \
  "v_cvt_pk_bf16_f32 v172, v172, 0\n" \
  "v_cvt_pk_bf16_f32 v173, 0, v173\n" \
  "v_mfma_f32_16x16x32_bf16 v[244:247], v[72:75], v[176:179], 0\n" \
  "v_cvt_pk_bf16_f32 v174, v174, 0\n" \
  "v_cvt_pk_bf16_f32 v175, 0, v175\n" \
  "v_mfma_f32_16x16x32_bf16 v[240:243], v[64:67], v[180:183], v[240:243]\n" \
  "v_mfma_f32_16x16x32_bf16 v[244:247], v[80:83], v[180:183], v[244:247]\n" \
  "v_mfma_f32_16x16x32_bf16 v[244:247], v[56:59], v[184:187], v[244:247]\n" \
  "ds_write_b128 v53, v[248:251] offset:16384\n" \
  "ds_write_b128 v53, v[252:255] offset:24576\n" \
  "v_mfma_f32_16x16x32_bf16 v[244:247], v[64:67], v[188:191], v[244:247]\n" \
  "s_nop 2\n" \
  "ds_write_b128 v53, v[240:243]\n" \
  "s_nop 3\n" \
  "ds_write_b128 v53, v[244:247] offset:8192\n" \
  "C_fy:\n" \
  "s_waitcnt vmcnt(1)\n" \
  "v_max3_u32 v32, v92, v93, v94\n" \
  "v_min3_u32 v34, v92, v93, v94\n" \
  "s_waitcnt vmcnt(0)\n" \
  "v_max3_u32 v32, v32, v95, s67\n" \
  "v_min3_u32 v34, v34, v95, s67\n" \
  "v_cmp_ne_u32 vcc, v32, v34\n" \
  "s_nop 0\n" \
  "s_cbranch_vccnz F_fy\n" \
  "K_fy:\n" \
  "CVJ:\n" \
  "s_waitcnt lgkmcnt(0)\n" \
  "s_barrier\n" \
  "ds_read_b128 v[56:59], v54\n" \
  "ds_read_b128 v[64:67], v54 offset:1024\n" \
  "ds_read_b128 v[72:75], v54 offset:2048\n" \
  "ds_read_b128 v[80:83], v54 offset:3072\n" \
  "ds_read_b128 v[88:91], v54 offset:256\n" \
  "ds_read_b128 v[96:99], v54 offset:1280\n" \
  "ds_read_b128 v[104:107], v54 offset:2304\n" \
  "ds_read_b128 v[112:115], v54 offset:3328\n" \
  "s_waitcnt lgkmcnt(6)\n" \
  "v_add_f32 v56, v56, v64\n" \
  "v_add_f32 v57, v57, v65\n" \
  "v_add_f32 v58, v58, v66\n" \
  "v_add_f32 v59, v59, v67\n" \
  "s_waitcnt lgkmcnt(4)\n" \
  "v_add_f32 v72, v72, v80\n" \
  "v_add_f32 v73, v73, v81\n" \
  "v_add_f32 v74, v74, v82\n" \
  "v_add_f32 v75, v75, v83\n" \
  "v_add_f32 v56, v56, v72\n" \
  "v_add_f32 v57, v57, v73\n" \
  "v_add_f32 v58, v58, v74\n" \
  "v_add_f32 v59, v59, v75\n" \
  "v_cvt_pk_bf16_f32 v56, 0, v56\n" \
  "v_cvt_pk_bf16_f32 v57, 0, v57\n" \
  "v_cvt_pk_bf16_f32 v58, 0, v58\n" \
  "v_cvt_pk_bf16_f32 v59, 0, v59\n" \
  "v_add_f32 v40, v40, v56\n" \
  "v_add_f32 v41, v41, v57\n" \
  "v_add_f32 v42, v42, v58\n" \
  "v_add_f32 v43, v43, v59\n" \
  "v_cvt_pk_bf16_f32 v120, v40, v41\n" \
  "v_cvt_pk_bf16_f32 v121, v42, v43\n" \
  "v_dot2_f32_bf16 v40, v120, v168, 0\n" \
  "v_dot2_f32_bf16 v41, v120, v169, 0\n" \
  "v_dot2_f32_bf16 v42, v121, v170, 0\n" \
  "v_dot2_f32_bf16 v43, v121, v171, 0\n" \
  "v_cvt_pk_bf16_f32 v40, 0, v40\n" \
  "v_cvt_pk_bf16_f32 v41, 0, v41\n" \
  "v_cvt_pk_bf16_f32 v42, 0, v42\n" \
  "v_cvt_pk_bf16_f32 v43, 0, v43\n" \
  "v_add_f32 v40, v40, v152\n" \
  "v_add_f32 v41, v41, v153\n" \
  "v_add_f32 v42, v42, v154\n" \
  "v_add_f32 v43, v43, v155\n" \
  "v_cvt_pk_bf16_f32 v128, v40, v41\n" \
  "v_cvt_pk_bf16_f32 v129, v42, v43\n" \
  "s_waitcnt lgkmcnt(2)\n" \
  "v_add_f32 v88, v88, v96\n" \
  "v_add_f32 v89, v89, v97\n" \
  "v_add_f32 v90, v90, v98\n" \
  "v_add_f32 v91, v91, v99\n" \
  "s_waitcnt lgkmcnt(0)\n" \
  "v_add_f32 v104, v104, v112\n" \
  "v_add_f32 v105, v105, v113\n" \
  "v_add_f32 v106, v106, v114\n" \
  "v_add_f32 v107, v107, v115\n" \
  "v_add_f32 v88, v88, v104\n" \
  "v_add_f32 v89, v89, v105\n" \
  "v_add_f32 v90, v90, v106\n" \
  "v_add_f32 v91, v91, v107\n" \
  "v_cvt_pk_bf16_f32 v88, 0, v88\n" \
  "v_cvt_pk_bf16_f32 v89, 0, v89\n" \
  "v_cvt_pk_bf16_f32 v90, 0, v90\n" \
  "v_cvt_pk_bf16_f32 v91, 0, v91\n" \
  "v_add_f32 v44, v44, v88\n" \
  "v_add_f32 v45, v45, v89\n" \
  "v_add_f32 v46, v46, v90\n" \
  "v_add_f32 v47, v47, v91\n" \
  "v_cvt_pk_bf16_f32 v122, v44, v45\n" \
  "v_cvt_pk_bf16_f32 v123, v46, v47\n" \
  "v_dot2_f32_bf16 v44, v122, v172, 0\n" \
  "v_dot2_f32_bf16 v45, v122, v173, 0\n" \
  "v_dot2_f32_bf16 v46, v123, v174, 0\n" \
  "v_dot2_f32_bf16 v47, v123, v175, 0\n" \
  "v_cvt_pk_bf16_f32 v44, 0, v44\n" \
  "v_cvt_pk_bf16_f32 v45, 0, v45\n" \
  "v_cvt_pk_bf16_f32 v46, 0, v46\n" \
  "v_cvt_pk_bf16_f32 v47, 0, v47\n" \
  "v_add_f32 v44, v44, v160\n" \
  "v_add_f32 v45, v45, v161\n" \
  "v_add_f32 v46, v46, v162\n" \
  "v_add_f32 v47, v47, v163\n" \
  "v_cvt_pk_bf16_f32 v130, v44, v45\n" \
  "v_cvt_pk_bf16_f32 v131, v46, v47\n" \
  "buffer_store_dwordx4 v[128:131], v38, s[28:31], s70 offen sc1\n" \
  "s_setprio 0\n" \
  "s_sub_u32 s70, s70, 0x390000\n" \
  "buffer_store_dwordx4 v[120:123], v38, s[28:31], s70 offen sc1\n" \
  "buffer_store_dwordx4 v[48:51], v0, s[28:31], s53 offen sc1\n" \
  "s_or_b32 s58, %[b], %[w]\n" \
  "s_cselect_b64 exec, 0, 1\n" \
  "buffer_store_dword v36, off, s[28:31], 0 sc1\n" \
  "s_mov_b64 exec, -1\n" \
  "s_cmp_gt_u32 %[w], 5\n" \
  "s_cbranch_scc1 WS0\n" \
  "v_add_u32 v52, s12, v0\n" \
  "v_mov_b32 v248, 0x3f803f80\n" \
  "v_mov_b32 v249, 0x3f803f80\n" \
  "v_mov_b32 v250, 0x3f803f80\n" \
  "v_mov_b32 v251, 0x3f803f80\n" \
  "s_lshl_b32 s58, %[w], 6\n" \
  "v_add_u32 v53, s58, v10\n" \
  "s_mov_b32 s58, 0x10001\n" \
  "s_mov_b32 s59, 0x10001\n" \
  "s_waitcnt vmcnt(17)\n" \
  "ds_write_b128 v52, v[92:95]\n" \
  "v_mfma_f32_16x16x32_bf16 v[44:47], v[92:95], v[248:251], 0\n" \
  "s_waitcnt vmcnt(16)\n" \
  "ds_write_b128 v52, v[100:103] offset:2048\n" \
  "v_mfma_f32_16x16x32_bf16 v[40:43], v[100:103], v[248:251], 0\n" \
  "s_waitcnt vmcnt(15)\n" \
  "ds_write_b128 v52, v[108:111] offset:4096\n" \
  "v_mfma_f32_16x16x32_bf16 v[44:47], v[108:111], v[248:251], v[44:47]\n" \
  "s_waitcnt vmcnt(14)\n" \
  "ds_write_b128 v52, v[116:119] offset:6144\n" \
  "v_mfma_f32_16x16x32_bf16 v[40:43], v[116:119], v[248:251], v[40:43]\n" \
  "s_waitcnt vmcnt(13)\n" \
  "ds_write_b128 v52, v[124:127] offset:8192\n" \
  "v_mfma_f32_16x16x32_bf16 v[44:47], v[124:127], v[248:251], v[44:47]\n" \
  "s_waitcnt vmcnt(12)\n" \
  "ds_write_b128 v52, v[132:135] offset:10240\n" \
  "v_mfma_f32_16x16x32_bf16 v[40:43], v[132:135], v[248:251], v[40:43]\n" \
  "s_waitcnt vmcnt(11)\n" \
  "ds_write_b128 v52, v[140:143] offset:12288\n" \
  "v_mfma_f32_16x16x32_bf16 v[44:47], v[140:143], v[248:251], v[44:47]\n" \
  "s_waitcnt vmcnt(10)\n" \
  "ds_write_b128 v52, v[148:151] offset:14336\n" \
  "v_mfma_f32_16x16x32_bf16 v[40:43], v[148:151], v[248:251], v[40:43]\n" \
  "s_waitcnt vmcnt(9)\n" \
  "ds_write_b128 v52, v[156:159] offset:16384\n" \
  "v_mfma_f32_16x16x32_bf16 v[44:47], v[156:159], v[248:251], v[44:47]\n" \
  "s_waitcnt vmcnt(8)\n" \
  "ds_write_b128 v52, v[164:167] offset:18432\n" \
  "v_mfma_f32_16x16x32_bf16 v[40:43], v[164:167], v[248:251], v[40:43]\n" \
  "s_waitcnt vmcnt(7)\n" \
  "ds_write_b128 v52, v[60:63] offset:20480\n" \
  "v_mfma_f32_16x16x32_bf16 v[44:47], v[60:63], v[248:251], v[44:47]\n" \
  "s_waitcnt vmcnt(6)\n" \
  "ds_write_b128 v52, v[232:235] offset:22528\n" \
  "v_mfma_f32_16x16x32_bf16 v[40:43], v[232:235], v[248:251], v[40:43]\n" \
  "s_waitcnt vmcnt(5)\n" \
  "ds_write_b128 v52, v[236:239] offset:24576\n" \
  "v_mfma_f32_16x16x32_bf16 v[44:47], v[236:239], v[248:251], v[44:47]\n" \
  "s_waitcnt vmcnt(4)\n" \
  "ds_write_b128 v52, v[136:139] offset:26624\n" \
  "v_mfma_f32_16x16x32_bf16 v[40:43], v[136:139], v[248:251], v[40:43]\n" \
  "s_nop 7\n" \
  "v_add_f32 v44, v44, v40\n" \
  "v_add_f32 v45, v45, v41\n" \
  "v_add_f32 v46, v46, v42\n" \
  "v_add_f32 v47, v47, v43\n" \
  "s_mov_b64 exec, s[58:59]\n" \
  "ds_write_b128 v53, v[44:47]\n" \
  "s_mov_b64 exec, -1\n" \
  "WS0:\n" \
  "s_waitcnt vmcnt(2)\n" \
  "s_waitcnt vmcnt(1)\n" \
  "PREP:\n" \
  "s_waitcnt lgkmcnt(0)\n" \
  "s_barrier\n" \
  "buffer_load_dwordx4 v[168:171], v1, s[28:31], s46 offen sc1\n" \
  "s_lshl_b32 s58, s66, 4\n" \
  "s_add_u32 s58, s58, 0x3000\n" \
  "s_cmp_eq_u32 %[w], 7\n" \
  "s_cselect_b64 exec, 1, 0\n" \
  "buffer_store_dword v36, off, s[28:31], s58 sc1\n" \
  "s_mov_b64 exec, -1\n" \
  "s_and_b32 s64, %[w], 3\n" \
  "s_lshl_b32 s64, s64, 10\n" \
  "s_add_u32 s64, s64, 0x3000\n" \
  "buffer_load_dwordx4 v[172:175], v1, s[28:31], s46 offen offset:1024 sc1\n" \
  "ds_read_b128 v[224:227], v0\n" \
  "ds_read_b128 v[228:231], v0 offset:1024\n" \
  "buffer_load_dwordx4 v[176:179], v1, s[28:31], s46 offen offset:2048 sc1\n" \
  "ds_read_b128 v[232:235], v0 offset:2048\n" \
  "ds_read_b128 v[236:239], v0 offset:3072\n" \
  "buffer_load_dwordx4 v[180:183], v1, s[28:31], s46 offen offset:3072 sc1\n" \
  "ds_read_b128 v[240:243], v0 offset:4096\n" \
  "ds_read_b128 v[244:247], v0 offset:5120\n" \
  "buffer_load_dwordx4 v[184:187], v1, s[28:31], s47 offen sc1\n" \
  "v_mov_b32 v28, -1.0\n" \
  "v_mov_b32 v29, 1.0\n" \
  "ds_write_b64 v8, v[28:29] offset:1024\n" \
  "v_mov_b32 v30, 1.0\n" \
  "v_mov_b32 v31, 1.0\n" \
  "buffer_load_dwordx4 v[188:191], v1, s[28:31], s47 offen offset:1024 sc1\n" \
  "v_mov_b32 v36, 0\n" \
  "v_mov_b32 v37, 0\n" \
  "v_mov_b32 v38, 0\n" \
  "v_mov_b32 v39, 0\n" \
  "v_mov_b32 v28, 0\n" \
  "v_mov_b32 v60, -1\n" \
  "v_mov_b32 v61, -1\n" \
  "v_mov_b32 v62, -1\n" \
  "v_mov_b32 v63, -1\n" \
  "buffer_load_dwordx4 v[192:195], v1, s[28:31], s47 offen offset:2048 sc1\n" \
  "buffer_load_dwordx4 v[196:199], v1, s[28:31], s47 offen offset:3072 sc1\n" \
  "buffer_load_dwordx4 v[200:203], v1, s[28:31], s48 offen sc1\n" \
  "buffer_load_dwordx4 v[204:207], v1, s[28:31], s48 offen offset:1024 sc1\n" \
  "s_setprio 2\n" \
  "C_y_0:\n" \
  "s_waitcnt vmcnt(8)\n" \
  "v_max3_u32 v34, v169, v171, v173\n" \
  "v_max_u32 v34, v34, v175\n" \
  "v_cmp_eq_u32 vcc, -1, v34\n" \
  "v_mfma_f32_16x16x32_bf16 v[252:255], v[168:171], v[16:19], 0\n" \
  "v_mfma_f32_16x16x32_bf16 v[40:43], v[168:171], v[20:23], 0\n" \
  "s_cbranch_vccnz F_y_0\n" \
  "K_y_0:\n" \
  "v_mfma_f32_16x16x32_bf16 v[248:251], v[172:175], v[16:19], 0\n" \
  "v_mfma_f32_16x16x32_bf16 v[76:79], v[172:175], v[20:23], 0\n" \
  "s_nop 3\n" \
  "v_cvt_pk_bf16_f32 v56, v252, v253\n" \
  "v_cvt_pk_bf16_f32 v57, v254, v255\n" \
  "v_cvt_pk_bf16_f32 v58, v40, v41\n" \
  "v_cvt_pk_bf16_f32 v59, v42, v43\n" \
  "buffer_load_dwordx4 v[208:211], v1, s[28:31], s48 offen offset:2048 sc1\n" \
  "s_nop 0\n" \
  "v_mfma_f32_16x16x32_bf16 v[44:47], v[56:59], v[56:59], 0\n" \
  "ds_read_b128 v[168:171], v0 offset:28672\n" \
  "ds_read_b128 v[124:127], v0 offset:49152\n" \
  "s_waitcnt vmcnt(7)\n" \
  "v_max3_u32 v34, v177, v179, v181\n" \
  "v_max_u32 v34, v34, v183\n" \
  "v_cmp_eq_u32 vcc, -1, v34\n" \
  "s_waitcnt lgkmcnt(7)\n" \
  "v_mfma_f32_16x16x32_bf16 v[52:55], v[228:231], v[56:59], 0\n" \
  "ds_read_b128 v[228:231], v0 offset:7168\n" \
  "v_cvt_pk_bf16_f32 v64, v248, v249\n" \
  "v_mfma_f32_16x16x32_bf16 v[48:51], v[224:227], v[56:59], 0\n" \
  "ds_read_b128 v[224:227], v0 offset:6144\n" \
  "v_cvt_pk_bf16_f32 v65, v250, v251\n" \
  "s_cbranch_vccnz F_y_2\n" \
  "K_y_2:\n" \
  "v_mfma_f32_16x16x32_bf16 v[252:255], v[176:179], v[16:19], 0\n" \
  "v_cvt_pk_bf16_f32 v66, v76, v77\n" \
  "v_cvt_pk_bf16_f32 v67, v78, v79\n" \
  "v_mfma_f32_16x16x32_bf16 v[40:43], v[176:179], v[20:23], 0\n" \
  "buffer_load_dwordx4 v[212:215], v1, s[28:31], s48 offen offset:3072 sc1\n" \
  "v_mfma_f32_16x16x32_bf16 v[44:47], v[64:67], v[64:67], v[44:47]\n" \
  "ds_read_b128 v[172:175], v0 offset:29696\n" \
  "ds_read_b128 v[132:135], v0 offset:50176\n" \
  "s_waitcnt lgkmcnt(9)\n" \
  "v_mfma_f32_16x16x32_bf16 v[52:55], v[236:239], v[64:67], v[52:55]\n" \
  "ds_read_b128 v[236:239], v0 offset:9216\n" \
  "v_cvt_pk_bf16_f32 v72, v252, v253\n" \
  "v_mfma_f32_16x16x32_bf16 v[48:51], v[232:235], v[64:67], v[48:51]\n" \
  "ds_read_b128 v[232:235], v0 offset:8192\n" \
  "v_cvt_pk_bf16_f32 v73, v254, v255\n" \
  "v_mfma_f32_16x16x32_bf16 v[248:251], v[180:183], v[16:19], 0\n" \
  "v_cvt_pk_bf16_f32 v74, v40, v41\n" \
  "v_cvt_pk_bf16_f32 v75, v42, v43\n" \
  "v_mfma_f32_16x16x32_bf16 v[76:79], v[180:183], v[20:23], 0\n" \
  "buffer_load_dwordx4 v[216:219], v1, s[28:31], s49 offen sc1\n" \
  "v_mfma_f32_16x16x32_bf16 v[44:47], v[72:75], v[72:75], v[44:47]\n" \
  "ds_read_b128 v[176:179], v0 offset:30720\n" \
  "s_waitcnt vmcnt(7)\n" \
  "v_max3_u32 v34, v185, v187, v189\n" \
  "v_max_u32 v34, v34, v191\n" \
  "v_cmp_eq_u32 vcc, -1, v34\n" \
  "s_waitcnt lgkmcnt(10)\n" \
  "v_mfma_f32_16x16x32_bf16 v[52:55], v[244:247], v[72:75], v[52:55]\n" \
  "ds_read_b128 v[244:247], v0 offset:11264\n" \
  "v_cvt_pk_bf16_f32 v80, v248, v249\n" \
  "v_mfma_f32_16x16x32_bf16 v[48:51], v[240:243], v[72:75], v[48:51]\n" \
  "ds_read_b128 v[240:243], v0 offset:10240\n" \
  "v_cvt_pk_bf16_f32 v81, v250, v251\n" \
  "s_cbranch_vccnz F_y_4\n" \
  "K_y_4:\n" \
  "v_mfma_f32_16x16x32_bf16 v[252:255], v[184:187], v[16:19], 0\n" \
  "v_cvt_pk_bf16_f32 v82, v76, v77\n" \
  "v_cvt_pk_bf16_f32 v83, v78, v79\n" \
  "v_mfma_f32_16x16x32_bf16 v[40:43], v[184:187], v[20:23], 0\n" \
  "buffer_load_dwordx4 v[220:223], v1, s[28:31], s49 offen offset:1024 sc1\n" \
  "v_mfma_f32_16x16x32_bf16 v[44:47], v[80:83], v[80:83], v[44:47]\n" \
  "ds_read_b128 v[180:183], v0 offset:31744\n" \
  "s_waitcnt lgkmcnt(9)\n" \
  "v_mfma_f32_16x16x32_bf16 v[52:55], v[228:231], v[80:83], v[52:55]\n" \
  "ds_read_b128 v[228:231], v0 offset:13312\n" \
  "v_cvt_pk_bf16_f32 v88, v252, v253\n" \
  "s_waitcnt lgkmcnt(9)\n" \
  "v_mfma_f32_16x16x32_bf16 v[48:51], v[224:227], v[80:83], v[48:51]\n" \
  "ds_read_b128 v[224:227], v0 offset:12288\n" \
  "v_cvt_pk_bf16_f32 v89, v254, v255\n" \
  "v_mfma_f32_16x16x32_bf16 v[248:251], v[188:191], v[16:19], 0\n" \
  "v_cvt_pk_bf16_f32 v90, v40, v41\n" \
  "v_cvt_pk_bf16_f32 v91, v42, v43\n" \
  "v_mfma_f32_16x16x32_bf16 v[76:79], v[188:191], v[20:23], 0\n" \
  "s_nop 0\n" \
  "v_mfma_f32_16x16x32_bf16 v[44:47], v[88:91], v[88:91], v[44:47]\n" \
  "ds_read_b128 v[184:187], v0 offset:32768\n" \
  "s_waitcnt vmcnt(6)\n" \
  "v_max3_u32 v34, v193, v195, v197\n" \
  "v_max_u32 v34, v34, v199\n" \
  "v_cmp_eq_u32 vcc, -1, v34\n" \
  "s_waitcnt lgkmcnt(8)\n" \
  "v_mfma_f32_16x16x32_bf16 v[52:55], v[236:239], v[88:91], v[52:55]\n" \
  "ds_read_b128 v[236:239], v0 offset:15360\n" \
  "v_cvt_pk_bf16_f32 v96, v248, v249\n" \
  "s_waitcnt lgkmcnt(8)\n" \
  "v_mfma_f32_16x16x32_bf16 v[48:51], v[232:235], v[88:91], v[48:51]\n" \
  "ds_read_b128 v[232:235], v0 offset:14336\n" \
  "v_cvt_pk_bf16_f32 v97, v250, v251\n" \
  "s_cbranch_vccnz F_y_6\n" \
  "K_y_6:\n" \
  "v_mfma_f32_16x16x32_bf16 v[252:255], v[192:195], v[16:19], 0\n" \
  "v_cvt_pk_bf16_f32 v98, v76, v77\n" \
  "v_cvt_pk_bf16_f32 v99, v78, v79\n" \
  "v_mfma_f32_16x16x32_bf16 v[40:43], v[192:195], v[20:23], 0\n" \
  "s_nop 0\n" \
  "v_mfma_f32_16x16x32_bf16 v[44:47], v[96:99], v[96:99], v[44:47]\n" \
  "ds_read_b128 v[188:191], v0 offset:33792\n" \
  "s_waitcnt lgkmcnt(8)\n" \
  "v_mfma_f32_16x16x32_bf16 v[52:55], v[244:247], v[96:99], v[52:55]\n" \
  "ds_read_b128 v[244:247], v0 offset:17408\n" \
  "v_cvt_pk_bf16_f32 v104, v252, v253\n" \
  "s_waitcnt lgkmcnt(8)\n" \
  "v_mfma_f32_16x16x32_bf16 v[48:51], v[240:243], v[96:99], v[48:51]\n" \
  "ds_read_b128 v[240:243], v0 offset:16384\n" \
  "v_cvt_pk_bf16_f32 v105, v254, v255\n" \
  "v_mfma_f32_16x16x32_bf16 v[248:251], v[196:199], v[16:19], 0\n" \
  "v_cvt_pk_bf16_f32 v106, v40, v41\n" \
  "v_cvt_pk_bf16_f32 v107, v42, v43\n" \
  "v_mfma_f32_16x16x32_bf16 v[76:79], v[196:199], v[20:23], 0\n" \
  "s_nop 0\n" \
  "v_mfma_f32_16x16x32_bf16 v[44:47], v[104:107], v[104:107], v[44:47]\n" \
  "ds_read_b128 v[192:195], v0 offset:34816\n" \
  "s_waitcnt vmcnt(4)\n" \
  "v_max3_u32 v34, v201, v203, v205\n" \
  "v_max_u32 v34, v34, v207\n" \
  "v_cmp_eq_u32 vcc, -1, v34\n" \
  "s_waitcnt lgkmcnt(8)\n" \
  "v_mfma_f32_16x16x32_bf16 v[52:55], v[228:231], v[104:107], v[52:55]\n" \
  "ds_read_b128 v[228:231], v0 offset:19456\n" \
  "v_cvt_pk_bf16_f32 v112, v248, v249\n" \
  "s_waitcnt lgkmcnt(8)\n" \
  "v_mfma_f32_16x16x32_bf16 v[48:51], v[224:227], v[104:107], v[48:51]\n" \
  "ds_read_b128 v[224:227], v0 offset:18432\n" \
  "v_cvt_pk_bf16_f32 v113, v250, v251\n" \
  "s_cbranch_vccnz F_y_8\n" \
  "K_y_8:\n" \
  "v_mfma_f32_16x16x32_bf16 v[252:255], v[200:203], v[16:19], 0\n" \
  "v_cvt_pk_bf16_f32 v114, v76, v77\n" \
  "v_cvt_pk_bf16_f32 v115, v78, v79\n" \
  "v_mfma_f32_16x16x32_bf16 v[40:43], v[200:203], v[20:23], 0\n" \
  "s_nop 0\n" \
  "v_mfma_f32_16x16x32_bf16 v[44:47], v[112:115], v[112:115], v[44:47]\n" \
  "ds_read_b128 v[196:199], v0 offset:35840\n" \
  "s_waitcnt lgkmcnt(8)\n" \
  "v_mfma_f32_16x16x32_bf16 v[52:55], v[236:239], v[112:115], v[52:55]\n" \
  "ds_read_b128 v[236:239], v0 offset:21504\n" \
  "v_cvt_pk_bf16_f32 v120, v252, v253\n" \
  "s_waitcnt lgkmcnt(8)\n" \
  "v_mfma_f32_16x16x32_bf16 v[48:51], v[232:235], v[112:115], v[48:51]\n" \
  "ds_read_b128 v[232:235], v0 offset:20480\n" \
  "v_cvt_pk_bf16_f32 v121, v254, v255\n" \
  "v_mfma_f32_16x16x32_bf16 v[248:251], v[204:207], v[16:19], 0\n" \
  "v_cvt_pk_bf16_f32 v122, v40, v41\n" \
  "v_cvt_pk_bf16_f32 v123, v42, v43\n" \
  "v_mfma_f32_16x16x32_bf16 v[76:79], v[204:207], v[20:23], 0\n" \
  "s_nop 0\n" \
  "v_mfma_f32_16x16x32_bf16 v[44:47], v[120:123], v[120:123], v[44:47]\n" \
  "ds_read_b128 v[200:203], v0 offset:36864\n" \
  "s_waitcnt vmcnt(2)\n" \
  "v_max3_u32 v34, v209, v211, v213\n" \
  "v_max_u32 v34, v34, v215\n" \
  "v_cmp_eq_u32 vcc, -1, v34\n" \
  "s_waitcnt lgkmcnt(8)\n" \
  "v_mfma_f32_16x16x32_bf16 v[52:55], v[244:247], v[120:123], v[52:55]\n" \
  "ds_read_b128 v[244:247], v0 offset:23552\n" \
  "v_cvt_pk_bf16_f32 v128, v248, v249\n" \
  "s_waitcnt lgkmcnt(8)\n" \
  "v_mfma_f32_16x16x32_bf16 v[48:51], v[240:243], v[120:123], v[48:51]\n" \
  "ds_read_b128 v[240:243], v0 offset:22528\n" \
  "v_cvt_pk_bf16_f32 v129, v250, v251\n" \
  "s_cbranch_vccnz F_y_10\n" \
  "K_y_10:\n" \
  "v_mfma_f32_16x16x32_bf16 v[252:255], v[208:211], v[16:19], 0\n" \
  "v_cvt_pk_bf16_f32 v130, v76, v77\n" \
  "v_cvt_pk_bf16_f32 v131, v78, v79\n" \
  "v_mfma_f32_16x16x32_bf16 v[40:43], v[208:211], v[20:23], 0\n" \
  "s_nop 0\n" \
  "v_mfma_f32_16x16x32_bf16 v[44:47], v[128:131], v[128:131], v[44:47]\n" \
  "ds_read_b128 v[204:207], v0 offset:37888\n" \
  "s_waitcnt lgkmcnt(8)\n" \
  "v_mfma_f32_16x16x32_bf16 v[52:55], v[228:231], v[128:131], v[52:55]\n" \
  "ds_read_b128 v[228:231], v0 offset:25600\n" \
  "v_cvt_pk_bf16_f32 v136, v252, v253\n" \
  "s_waitcnt lgkmcnt(8)\n" \
  "v_mfma_f32_16x16x32_bf16 v[48:51], v[224:227], v[128:131], v[48:51]\n" \
  "ds_read_b128 v[224:227], v0 offset:24576\n" \
  "v_cvt_pk_bf16_f32 v137, v254, v255\n" \
  "v_mfma_f32_16x16x32_bf16 v[248:251], v[212:215], v[16:19], 0\n" \
  "v_cvt_pk_bf16_f32 v138, v40, v41\n" \
  "v_cvt_pk_bf16_f32 v139, v42, v43\n" \
  "v_mfma_f32_16x16x32_bf16 v[76:79], v[212:215], v[20:23], 0\n" \
  "v_mov_b32 v84, s67\n" \
  "v_mov_b32 v85, s67\n" \
  "v_mov_b32 v86, s67\n" \
  "v_mov_b32 v87, s67\n" \
  "s_add_u32 s59, s50, 0x600000\n" \
  "buffer_store_dwordx4 v[84:87], v0, s[28:31], s59 offen sc1\n" \
  "s_add_u32 s59, s50, 0x600000\n" \
  "buffer_store_dwordx4 v[84:87], v26, s[28:31], s59 offen sc1\n" \
  "v_mfma_f32_16x16x32_bf16 v[44:47], v[136:139], v[136:139], v[44:47]\n" \
  "ds_read_b128 v[208:211], v0 offset:38912\n" \
  "s_waitcnt vmcnt(2)\n" \
  "v_max3_u32 v34, v217, v219, v221\n" \
  "v_max_u32 v34, v34, v223\n" \
  "v_cmp_eq_u32 vcc, -1, v34\n" \
  "s_waitcnt lgkmcnt(8)\n" \
  "v_mfma_f32_16x16x32_bf16 v[52:55], v[236:239], v[136:139], v[52:55]\n" \
  "ds_read_b128 v[236:239], v0 offset:27648\n" \
  "v_cvt_pk_bf16_f32 v144, v248, v249\n" \
  "s_waitcnt lgkmcnt(8)\n" \
  "v_mfma_f32_16x16x32_bf16 v[48:51], v[232:235], v[136:139], v[48:51]\n" \
  "ds_read_b128 v[232:235], v0 offset:26624\n" \
  "v_cvt_pk_bf16_f32 v145, v250, v251\n" \
  "s_cbranch_vccnz F_y_12\n" \
  "K_y_12:\n" \
  "v_mfma_f32_16x16x32_bf16 v[252:255], v[216:219], v[16:19], 0\n" \
  "v_cvt_pk_bf16_f32 v146, v76, v77\n" \
  "v_cvt_pk_bf16_f32 v147, v78, v79\n" \
  "v_mfma_f32_16x16x32_bf16 v[40:43], v[216:219], v[20:23], 0\n" \
  "v_and_b32 v68, 0x70, v0\n" \
  "v_and_b32 v69, 0x80, v0\n" \
  "v_lshl_add_u32 v68, v69, 2, v68\n" \
  "s_cmp_eq_u32 %[w], 7\n" \
  "s_cselect_b64 vcc, -1, 0\n" \
  "s_cselect_b32 s58, s41, s38\n" \
  "v_cndmask_b32 v68, v13, v68, vcc\n" \
  "buffer_store_dwordx4 v[60:63], v68, s[28:31], s58 offen sc1\n" \
  "v_mfma_f32_16x16x32_bf16 v[44:47], v[144:147], v[144:147], v[44:47]\n" \
  "ds_read_b128 v[212:215], v0 offset:39936\n" \
  "s_waitcnt lgkmcnt(8)\n" \
  "v_mfma_f32_16x16x32_bf16 v[52:55], v[244:247], v[144:147], v[52:55]\n" \
  "ds_read_b128 v[244:247], v0 offset:44032\n" \
  "v_cvt_pk_bf16_f32 v152, v252, v253\n" \
  "s_waitcnt lgkmcnt(8)\n" \
  "v_mfma_f32_16x16x32_bf16 v[48:51], v[240:243], v[144:147], v[48:51]\n" \
  "ds_read_b128 v[240:243], v0 offset:43008\n" \
  "v_cvt_pk_bf16_f32 v153, v254, v255\n" \
  "v_mfma_f32_16x16x32_bf16 v[248:251], v[220:223], v[16:19], 0\n" \
  "v_cvt_pk_bf16_f32 v154, v40, v41\n" \
  "v_cvt_pk_bf16_f32 v155, v42, v43\n" \
  "v_mfma_f32_16x16x32_bf16 v[76:79], v[220:223], v[20:23], 0\n" \
  "s_nop 0\n" \
  "v_mfma_f32_16x16x32_bf16 v[44:47], v[152:155], v[152:155], v[44:47]\n" \
  "ds_read_b128 v[216:219], v0 offset:40960\n" \
  "s_waitcnt lgkmcnt(8)\n" \
  "v_mfma_f32_16x16x32_bf16 v[52:55], v[228:231], v[152:155], v[52:55]\n" \
  "ds_read_b128 v[228:231], v0 offset:46080\n" \
  "v_cvt_pk_bf16_f32 v160, v248, v249\n" \
  "s_waitcnt lgkmcnt(8)\n" \
  "v_mfma_f32_16x16x32_bf16 v[48:51], v[224:227], v[152:155], v[48:51]\n" \
  "ds_read_b128 v[224:227], v0 offset:45056\n" \
  "v_cvt_pk_bf16_f32 v161, v250, v251\n" \
  "v_cvt_pk_bf16_f32 v162, v76, v77\n" \
  "v_cvt_pk_bf16_f32 v163, v78, v79\n" \
  "s_nop 1\n" \
  "v_mfma_f32_16x16x32_bf16 v[44:47], v[160:163], v[160:163], v[44:47]\n" \
  "ds_read_b128 v[220:223], v0 offset:41984\n" \
  "s_waitcnt lgkmcnt(8)\n" \
  "v_mfma_f32_16x16x32_bf16 v[52:55], v[236:239], v[160:163], v[52:55]\n" \
  "ds_read_b128 v[236:239], v0 offset:48128\n" \
  "s_waitcnt lgkmcnt(8)\n" \
  "v_mfma_f32_16x16x32_bf16 v[48:51], v[232:235], v[160:163], v[48:51]\n" \
  "ds_read_b128 v[232:235], v0 offset:47104\n" \
  "s_add_u32 s46, s37, 0x400000\n" \
  "s_add_u32 s47, s37, 0x401000\n" \
  "s_add_u32 s48, s37, 0x402000\n" \
  "s_add_u32 s49, s37, 0x403000\n" \
  "s_cmp_lt_u32 %[w], 0\n" \
  "s_cselect_b32 s68, 1, 2\n" \
  "COMMON:\n" \
  "v_mfma_f32_16x16x32_bf16 v[60:63], v[56:59], v[168:171], 0\n" \
  "ds_read_b128 v[76:79], v10\n" \
  "ds_read_b128 v[84:87], v10 offset:64\n" \
  "v_cndmask_b32 v102, 0, v44, s[44:45]\n" \
  "s_lshl_b64 vcc, s[44:45], 1\n" \
  "v_cndmask_b32 v102, v102, v45, vcc\n" \
  "s_lshl_b64 vcc, s[44:45], 2\n" \
  "v_cndmask_b32 v102, v102, v46, vcc\n" \
  "s_lshl_b64 vcc, s[44:45], 3\n" \
  "v_cndmask_b32 v102, v102, v47, vcc\n" \
  "v_cndmask_b32 v103, 0, v52, s[60:61]\n" \
  "s_nop 1\n" \
  "v_permlane32_swap_b32 v102, v103\n" \
  "v_add_f32 v102, v102, v103\n" \
  "v_mov_b32 v103, v102\n" \
  "s_nop 1\n" \
  "v_permlane16_swap_b32 v102, v103\n" \
  "v_add_f32 v102, v102, v103\n" \
  "v_mov_b32 v103, v102\n" \
  "s_nop 1\n" \
  "v_permlane32_swap_b32 v102, v103\n" \
  "v_mfma_f32_16x16x32_bf16 v[68:71], v[56:59], v[172:175], 0\n" \
  "ds_read_b128 v[168:171], v0 offset:51200\n" \
  "v_mul_f32 v103, 0x3b124925, v103\n" \
  "v_mul_f32 v102, 0x3b124925, v102\n" \
  "v_fma_f32 v102, -v103, v103, v102\n" \
  "v_fma_f32 v103, v29, v103, v28\n" \
  "v_max_f32 v102, 0, v102\n" \
  "v_mul_f32 v102, v31, v102\n" \
  "v_cvt_pk_bf16_f32 v103, 0, v103\n" \
  "v_cvt_pk_bf16_f32 v102, 0, v102\n" \
  "v_add_f32 v33, 0x3727c5ac, v102\n" \
  "v_sub_f32 v103, v103, v28\n" \
  "v_mul_f32 v32, v103, v30\n" \
  "v_cvt_pk_bf16_f32 v33, 0, v33\n" \
  "s_waitcnt lgkmcnt(2)\n" \
  "v_fma_f32 v48, -v32, v76, v48\n" \
  "v_fma_f32 v49, -v32, v77, v49\n" \
  "v_sqrt_f32 v33, v33\n" \
  "v_fma_f32 v50, -v32, v78, v50\n" \
  "v_mfma_f32_16x16x32_bf16 v[60:63], v[64:67], v[176:179], v[60:63]\n" \
  "ds_read_b128 v[172:175], v0 offset:52224\n" \
  "v_fma_f32 v51, -v32, v79, v51\n" \
  "v_cvt_pk_bf16_f32 v33, 0, v33\n" \
  "s_waitcnt lgkmcnt(2)\n" \
  "v_fma_f32 v52, -v32, v84, v52\n" \
  "v_fma_f32 v53, -v32, v85, v53\n" \
  "v_rcp_f32 v33, v33\n" \
  "v_fma_f32 v54, -v32, v86, v54\n" \
  "v_fma_f32 v55, -v32, v87, v55\n" \
  "v_mul_f32 v33, v33, v29\n" \
  "v_mul_f32 v48, v33, v48\n" \
  "v_mul_f32 v49, v33, v49\n" \
  "v_mul_f32 v50, v33, v50\n" \
  "v_mul_f32 v51, v33, v51\n" \
  "v_cvt_pk_bf16_f32 v92, v48, v49\n" \
  "v_cvt_pk_bf16_f32 v93, v50, v51\n" \
  "v_mul_f32 v52, v33, v52\n" \
  "v_mul_f32 v53, v33, v53\n" \
  "v_mfma_f32_16x16x32_bf16 v[68:71], v[64:67], v[180:183], v[68:71]\n" \
  "ds_read_b128 v[176:179], v0 offset:53248\n" \
  "v_mul_f32 v54, v33, v54\n" \
  "v_mul_f32 v55, v33, v55\n" \
  "v_cvt_pk_bf16_f32 v94, v52, v53\n" \
  "v_cvt_pk_bf16_f32 v95, v54, v55\n" \
  "buffer_store_dwordx4 v[92:95], v0, s[28:31], s50 offen sc1\n" \
  "s_setprio 1\n" \
  "ds_write_b64 v8, v[32:33]\n" \
  "ds_write_b128 v3, v[92:95] offset:16384\n" \
  "ds_read_b128 v[76:79], v10 offset:256\n" \
  "ds_read_b128 v[84:87], v10 offset:320\n" \
  "ds_read_b32 v100, v11 offset:128\n" \
  "ds_read_b32 v101, v11 offset:192\n" \
  "v_mfma_f32_16x16x32_bf16 v[60:63], v[72:75], v[184:187], v[60:63]\n" \
  "ds_read_b128 v[180:183], v0 offset:54272\n" \
  "v_mov_b32 v35, 0xff800000\n" \
  "v_cmp_le_i32 vcc, 0, v27\n" \
  "v_mfma_f32_16x16x32_bf16 v[68:71], v[72:75], v[188:191], v[68:71]\n" \
  "ds_read_b128 v[184:187], v0 offset:55296\n" \
  "v_cmp_le_i32 s[58:59], 1, v27\n" \
  "v_mfma_f32_16x16x32_bf16 v[60:63], v[80:83], v[192:195], v[60:63]\n" \
  "ds_read_b128 v[188:191], v0 offset:56320\n" \
  "v_cndmask_b32 v156, 0, v35, vcc\n" \
  "v_cmp_le_i32 vcc, 2, v27\n" \
  "v_mfma_f32_16x16x32_bf16 v[68:71], v[80:83], v[196:199], v[68:71]\n" \
  "v_cndmask_b32 v157, 0, v35, s[58:59]\n" \
  "v_cmp_le_i32 s[58:59], 3, v27\n" \
  "v_mfma_f32_16x16x32_bf16 v[60:63], v[88:91], v[200:203], v[60:63]\n" \
  "ds_read_b128 v[192:195], v7\n" \
  "v_xor_b32 v164, v35, v156\n" \
  "v_cndmask_b32 v158, 0, v35, vcc\n" \
  "v_mfma_f32_16x16x32_bf16 v[68:71], v[88:91], v[204:207], v[68:71]\n" \
  "ds_read_b128 v[196:199], v7 offset:1024\n" \
  "v_xor_b32 v165, v35, v157\n" \
  "v_cndmask_b32 v159, 0, v35, s[58:59]\n" \
  "v_mfma_f32_16x16x32_bf16 v[60:63], v[96:99], v[208:211], v[60:63]\n" \
  "ds_read_b128 v[200:203], v7 offset:2048\n" \
  "v_xor_b32 v166, v35, v158\n" \
  "v_xor_b32 v167, v35, v159\n" \
  "v_mfma_f32_16x16x32_bf16 v[68:71], v[96:99], v[212:215], v[68:71]\n" \
  "ds_read_b128 v[204:207], v7 offset:3072\n" \
  "v_mov_b32 v102, 0x3f803f80\n" \
  "v_mul_f32 v33, 0x3e8b97f6, v33\n" \
  "v_mfma_f32_16x16x32_bf16 v[60:63], v[104:107], v[216:219], v[60:63]\n" \
  "ds_read_b128 v[208:211], v7 offset:4096\n" \
  "s_waitcnt lgkmcnt(11)\n" \
  "v_mul_f32 v48, -v32, v76\n" \
  "v_mul_f32 v49, -v32, v77\n" \
  "v_mfma_f32_16x16x32_bf16 v[68:71], v[104:107], v[220:223], v[68:71]\n" \
  "v_mul_f32 v50, -v32, v78\n" \
  "v_mul_f32 v51, -v32, v79\n" \
  "v_mfma_f32_16x16x32_bf16 v[60:63], v[112:115], v[240:243], v[60:63]\n" \
  "ds_read_b128 v[212:215], v7 offset:5120\n" \
  "s_waitcnt lgkmcnt(11)\n" \
  "v_mul_f32 v52, -v32, v84\n" \
  "v_mul_f32 v53, -v32, v85\n" \
  "v_mfma_f32_16x16x32_bf16 v[68:71], v[112:115], v[244:247], v[68:71]\n" \
  "ds_read_b128 v[216:219], v7 offset:6144\n" \
  "v_mul_f32 v54, -v32, v86\n" \
  "v_mul_f32 v55, -v32, v87\n" \
  "v_mfma_f32_16x16x32_bf16 v[60:63], v[120:123], v[224:227], v[60:63]\n" \
  "ds_read_b128 v[220:223], v7 offset:7168\n" \
  "ds_read_b128 v[44:47], v9\n" \
  "ds_read_b128 v[92:95], v9 offset:16\n" \
  "v_mfma_f32_16x16x32_bf16 v[68:71], v[120:123], v[228:231], v[68:71]\n" \
  "ds_read_b128 v[240:243], v7 offset:8192\n" \
  "v_mfma_f32_16x16x32_bf16 v[60:63], v[128:131], v[232:235], v[60:63]\n" \
  "ds_read_b128 v[244:247], v7 offset:9216\n" \
  "v_mfma_f32_16x16x32_bf16 v[68:71], v[128:131], v[236:239], v[68:71]\n" \
  "ds_read_b128 v[224:227], v7 offset:10240\n" \
  "v_mfma_f32_16x16x32_bf16 v[60:63], v[136:139], v[124:127], v[60:63]\n" \
  "v_mfma_f32_16x16x32_bf16 v[68:71], v[136:139], v[132:135], v[68:71]\n" \
  "ds_read_b128 v[228:231], v7 offset:11264\n" \
  "v_mfma_f32_16x16x32_bf16 v[60:63], v[144:147], v[168:171], v[60:63]\n" \
  "ds_read_b128 v[232:235], v7 offset:12288\n" \
  "v_mfma_f32_16x16x32_bf16 v[68:71], v[144:147], v[172:175], v[68:71]\n" \
  "ds_read_b128 v[236:239], v7 offset:13312\n" \
  "s_waitcnt lgkmcnt(15)\n" \
  "v_mfma_f32_16x16x32_bf16 v[60:63], v[152:155], v[176:179], v[60:63]\n" \
  "ds_read_b128 v[124:127], v7 offset:14336\n" \
  "v_mfma_f32_16x16x32_bf16 v[68:71], v[152:155], v[180:183], v[68:71]\n" \
  "ds_read_b128 v[132:135], v7 offset:15360\n" \
  "v_mfma_f32_16x16x32_bf16 v[60:63], v[160:163], v[184:187], v[60:63]\n" \
  "v_mfma_f32_16x16x32_bf16 v[68:71], v[160:163], v[188:191], v[68:71]\n" \
  "ds_read_b128 v[168:171], v7 offset:16384\n" \
  "s_waitcnt lgkmcnt(15)\n" \
  "v_mfma_f32_16x16x32_bf16 v[48:51], v[192:195], v[56:59], v[48:51]\n" \
  "ds_read_b128 v[172:175], v7 offset:17408\n" \
  "v_mfma_f32_16x16x32_bf16 v[52:55], v[196:199], v[56:59], v[52:55]\n" \
  "ds_read_b128 v[176:179], v7 offset:18432\n" \
  "v_mfma_f32_16x16x32_bf16 v[48:51], v[200:203], v[64:67], v[48:51]\n" \
  "ds_read_b128 v[180:183], v7 offset:19456\n" \
  "s_waitcnt lgkmcnt(13)\n" \
  "v_fma_f32 v60, -v44, v100, v60\n" \
  "v_fma_f32 v61, -v46, v100, v61\n" \
  "s_waitcnt lgkmcnt(12)\n" \
  "v_fma_f32 v62, -v92, v100, v62\n" \
  "v_fma_f32 v63, -v94, v100, v63\n" \
  "v_mfma_f32_16x16x32_bf16 v[52:55], v[204:207], v[64:67], v[52:55]\n" \
  "ds_read_b128 v[184:187], v7 offset:20480\n" \
  "v_mul_f32 v60, v45, v60\n" \
  "v_mul_f32 v61, v47, v61\n" \
  "v_mul_f32 v62, v93, v62\n" \
  "v_mul_f32 v63, v95, v63\n" \
  "v_mfma_f32_16x16x32_bf16 v[48:51], v[208:211], v[72:75], v[48:51]\n" \
  "ds_read_b128 v[188:191], v7 offset:21504\n" \
  "v_cvt_pk_bf16_f32 v76, v60, v61\n" \
  "v_cvt_pk_bf16_f32 v77, v62, v63\n" \
  "v_fma_f32 v68, -v44, v101, v68\n" \
  "v_fma_f32 v69, -v46, v101, v69\n" \
  "v_mfma_f32_16x16x32_bf16 v[52:55], v[212:215], v[72:75], v[52:55]\n" \
  "v_fma_f32 v70, -v92, v101, v70\n" \
  "v_fma_f32 v71, -v94, v101, v71\n" \
  "v_mul_f32 v68, v45, v68\n" \
  "v_mul_f32 v69, v47, v69\n" \
  "v_mfma_f32_16x16x32_bf16 v[48:51], v[216:219], v[80:83], v[48:51]\n" \
  "ds_read_b128 v[192:195], v7 offset:22528\n" \
  "buffer_load_dwordx4 v[108:111], v0, s[28:31], s51 offen sc1\n" \
  "buffer_load_dwordx4 v[116:119], v0, s[28:31], s52 offen sc1\n" \
  "buffer_load_dword v140, v0, s[28:31], s64 offen sc1\n" \
  "v_mul_f32 v70, v93, v70\n" \
  "v_mul_f32 v71, v95, v71\n" \
  "v_cvt_pk_bf16_f32 v78, v68, v69\n" \
  "v_cvt_pk_bf16_f32 v79, v70, v71\n" \
  "v_mfma_f32_16x16x32_bf16 v[52:55], v[220:223], v[80:83], v[52:55]\n" \
  "ds_read_b128 v[196:199], v7 offset:23552\n" \
  "v_cndmask_b32 v78, v78, v102, s[62:63]\n" \
  "v_cndmask_b32 v79, v79, v102, s[62:63]\n" \
  "s_waitcnt vmcnt(5)\n" \
  "s_waitcnt lgkmcnt(12)\n" \
  "v_mfma_f32_16x16x32_bf16 v[48:51], v[240:243], v[88:91], v[48:51]\n" \
  "ds_read_b128 v[200:203], v7 offset:24576\n" \
  "buffer_store_dwordx4 v[76:79], v26, s[28:31], s50 offen sc1\n" \
  "s_setprio 0\n" \
  "ds_write_b64 v5, v[76:77] offset:16384\n" \
  "ds_write_b64 v5, v[78:79] offset:17408\n" \
  "v_mfma_f32_16x16x32_bf16 v[52:55], v[244:247], v[88:91], v[52:55]\n" \
  "ds_read_b128 v[204:207], v7 offset:25600\n" \
  "v_mfma_f32_16x16x32_bf16 v[48:51], v[224:227], v[96:99], v[48:51]\n" \
  "ds_read_b128 v[208:211], v7 offset:26624\n" \
  "v_mfma_f32_16x16x32_bf16 v[52:55], v[228:231], v[96:99], v[52:55]\n" \
  "s_waitcnt lgkmcnt(13)\n" \
  "v_mfma_f32_16x16x32_bf16 v[48:51], v[232:235], v[104:107], v[48:51]\n" \
  "ds_read_b128 v[212:215], v7 offset:27648\n" \
  "v_mfma_f32_16x16x32_bf16 v[52:55], v[236:239], v[104:107], v[52:55]\n" \
  "v_mfma_f32_16x16x32_bf16 v[48:51], v[124:127], v[112:115], v[48:51]\n" \
  "v_mfma_f32_16x16x32_bf16 v[52:55], v[132:135], v[112:115], v[52:55]\n" \
  "s_waitcnt lgkmcnt(10)\n" \
  "v_mfma_f32_16x16x32_bf16 v[48:51], v[168:171], v[120:123], v[48:51]\n" \
  "v_mfma_f32_16x16x32_bf16 v[52:55], v[172:175], v[120:123], v[52:55]\n" \
  "v_mfma_f32_16x16x32_bf16 v[48:51], v[176:179], v[128:131], v[48:51]\n" \
  "v_mfma_f32_16x16x32_bf16 v[52:55], v[180:183], v[128:131], v[52:55]\n" \
  "s_waitcnt lgkmcnt(6)\n" \
  "v_mfma_f32_16x16x32_bf16 v[48:51], v[184:187], v[136:139], v[48:51]\n" \
  "v_mfma_f32_16x16x32_bf16 v[52:55], v[188:191], v[136:139], v[52:55]\n" \
  "v_mfma_f32_16x16x32_bf16 v[48:51], v[192:195], v[144:147], v[48:51]\n" \
  "v_mfma_f32_16x16x32_bf16 v[52:55], v[196:199], v[144:147], v[52:55]\n" \
  "s_waitcnt lgkmcnt(0)\n" \
  "v_mfma_f32_16x16x32_bf16 v[48:51], v[200:203], v[152:155], v[48:51]\n" \
  "v_mfma_f32_16x16x32_bf16 v[52:55], v[204:207], v[152:155], v[52:55]\n" \
  "v_mfma_f32_16x16x32_bf16 v[48:51], v[208:211], v[160:163], v[48:51]\n" \
  "v_mfma_f32_16x16x32_bf16 v[52:55], v[212:215], v[160:163], v[52:55]\n" \
  "C_nk:\n" \
  "s_waitcnt vmcnt(2)\n" \
  "v_max3_u32 v34, v109, v111, v117\n" \
  "v_max_u32 v34, v34, v119\n" \
  "v_cmp_eq_u32 vcc, s67, v34\n" \
  "s_nop 0\n" \
  "s_cbranch_vccnz F_nk\n" \
  "K_nk:\n" \
  "C_f2:\n" \
  "s_waitcnt vmcnt(1)\n" \
  "v_cmp_ne_u32 vcc, s67, v140\n" \
  "s_nop 0\n" \
  "s_cbranch_vccnz F_f2\n" \
  "K_f2:\n" \
  "s_cmp_gt_u32 s36, 1\n" \
  "s_cbranch_scc1 NKJ\n" \
  "v_mov_b32 v102, 0xf1490000\n" \
  "v_mov_b32 v116, 0\n" \
  "v_mov_b32 v117, 0\n" \
  "v_mov_b32 v119, 0\n" \
  "v_cndmask_b32 v118, 0, v102, s[60:61]\n" \
  "s_cmp_eq_u32 s36, 1\n" \
  "s_cbranch_scc1 NKJ\n" \
  "v_mov_b32 v108, v116\n" \
  "v_mov_b32 v109, v117\n" \
  "v_mov_b32 v110, v118\n" \
  "v_mov_b32 v111, v119\n" \
  "NKJ:\n" \
  "ds_write_b128 v3, v[108:111] offset:8192\n" \
  "ds_write_b128 v3, v[116:119]\n" \
  "s_setprio 1\n" \
  "s_nop 0 ;SEG\n" \
  "s_waitcnt lgkmcnt(0)\n" \
  "s_barrier\n" \
  "ds_read_b128 v[56:59], v3\n" \
  "ds_read_b128 v[60:63], v3 offset:1024\n" \
  "ds_read_b128 v[64:67], v3 offset:2048\n" \
  "ds_read_b128 v[68:71], v3 offset:3072\n" \
  "ds_read_b128 v[72:75], v3 offset:4096\n" \
  "ds_read_b128 v[76:79], v3 offset:5120\n" \
  "ds_read_b128 v[80:83], v3 offset:6144\n" \
  "ds_read_b128 v[84:87], v3 offset:7168\n" \
  "v_mul_f32 v52, v33, v52\n" \
  "v_mul_f32 v53, v33, v53\n" \
  "v_mul_f32 v54, v33, v54\n" \
  "v_mul_f32 v55, v33, v55\n" \
  "v_cvt_pk_bf16_f32 v250, v52, v53\n" \
  "v_cvt_pk_bf16_f32 v251, v54, v55\n" \
  "v_mul_f32 v48, v33, v48\n" \
  "v_mul_f32 v49, v33, v49\n" \
  "v_mul_f32 v50, v33, v50\n" \
  "v_mul_f32 v51, v33, v51\n" \
  "v_cvt_pk_bf16_f32 v248, v48, v49\n" \
  "v_cvt_pk_bf16_f32 v249, v50, v51\n" \
  "v_cndmask_b32 v250, v250, 1.0, s[60:61]\n" \
  "s_waitcnt lgkmcnt(7)\n" \
  "s_nop 0\n" \
  "v_mfma_f32_16x16x32_bf16 v[124:127], v[56:59], v[248:251], v[156:159]\n" \
  "ds_read_b128 v[88:91], v3 offset:8192\n" \
  "s_waitcnt lgkmcnt(7)\n" \
  "v_mfma_f32_16x16x32_bf16 v[128:131], v[60:63], v[248:251], 0\n" \
  "ds_read_b128 v[92:95], v3 offset:9216\n" \
  "s_waitcnt lgkmcnt(7)\n" \
  "v_mfma_f32_16x16x32_bf16 v[132:135], v[64:67], v[248:251], 0\n" \
  "ds_read_b128 v[96:99], v3 offset:10240\n" \
  "buffer_load_dwordx4 v[140:143], v26, s[28:31], s51 offen sc1\n" \
  "buffer_load_dwordx4 v[144:147], v26, s[28:31], s52 offen sc1\n" \
  "buffer_load_dwordx4 v[40:43], v12, s[28:31], s55 offen sc1\n" \
  "s_waitcnt lgkmcnt(7)\n" \
  "v_mfma_f32_16x16x32_bf16 v[136:139], v[68:71], v[248:251], 0\n" \
  "ds_read_b128 v[100:103], v3 offset:11264\n" \
  "v_max_f32 v35, v124, v125\n" \
  "v_max_f32 v32, v126, v127\n" \
  "s_waitcnt lgkmcnt(7)\n" \
  "v_mfma_f32_16x16x32_bf16 v[124:127], v[72:75], v[248:251], 0\n" \
  "ds_read_b128 v[104:107], v3 offset:12288\n" \
  "v_max3_f32 v35, v128, v129, v35\n" \
  "v_max3_f32 v32, v130, v131, v32\n" \
  "s_waitcnt lgkmcnt(7)\n" \
  "v_mfma_f32_16x16x32_bf16 v[128:131], v[76:79], v[248:251], 0\n" \
  "ds_read_b128 v[108:111], v3 offset:13312\n" \
  "v_max3_f32 v35, v132, v133, v35\n" \
  "v_max3_f32 v32, v134, v135, v32\n" \
  "s_waitcnt lgkmcnt(7)\n" \
  "v_mfma_f32_16x16x32_bf16 v[132:135], v[80:83], v[248:251], 0\n" \
  "ds_read_b128 v[112:115], v3 offset:14336\n" \
  "v_max3_f32 v35, v136, v137, v35\n" \
  "v_max3_f32 v32, v138, v139, v32\n" \
  "s_waitcnt lgkmcnt(7)\n" \
  "v_mfma_f32_16x16x32_bf16 v[136:139], v[84:87], v[248:251], 0\n" \
  "ds_read_b128 v[116:119], v3 offset:15360\n" \
  "v_max3_f32 v35, v124, v125, v35\n" \
  "v_max3_f32 v32, v126, v127, v32\n" \
  "s_waitcnt lgkmcnt(7)\n" \
  "v_mfma_f32_16x16x32_bf16 v[124:127], v[88:91], v[248:251], 0\n" \
  "ds_read_b128 v[120:123], v3 offset:16384\n" \
  "v_max3_f32 v35, v128, v129, v35\n" \
  "v_max3_f32 v32, v130, v131, v32\n" \
  "s_waitcnt lgkmcnt(7)\n" \
  "v_mfma_f32_16x16x32_bf16 v[128:131], v[92:95], v[248:251], 0\n" \
  "v_max3_f32 v35, v132, v133, v35\n" \
  "v_max3_f32 v32, v134, v135, v32\n" \
  "s_waitcnt lgkmcnt(6)\n" \
  "v_mfma_f32_16x16x32_bf16 v[132:135], v[96:99], v[248:251], 0\n" \
  "v_max3_f32 v35, v136, v137, v35\n" \
  "v_max3_f32 v32, v138, v139, v32\n" \
  "s_waitcnt lgkmcnt(5)\n" \
  "v_mfma_f32_16x16x32_bf16 v[136:139], v[100:103], v[248:251], 0\n" \
  "v_max3_f32 v35, v124, v125, v35\n" \
  "v_max3_f32 v32, v126, v127, v32\n" \
  "s_waitcnt lgkmcnt(4)\n" \
  "v_mfma_f32_16x16x32_bf16 v[124:127], v[104:107], v[248:251], 0\n" \
  "v_max3_f32 v35, v128, v129, v35\n" \
  "v_max3_f32 v32, v130, v131, v32\n" \
  "s_waitcnt lgkmcnt(3)\n" \
  "v_mfma_f32_16x16x32_bf16 v[128:131], v[108:111], v[248:251], 0\n" \
  "v_max3_f32 v35, v132, v133, v35\n" \
  "v_max3_f32 v32, v134, v135, v32\n" \
  "s_waitcnt lgkmcnt(2)\n" \
  "v_mfma_f32_16x16x32_bf16 v[132:135], v[112:115], v[248:251], 0\n" \
  "v_max3_f32 v35, v136, v137, v35\n" \
  "v_max3_f32 v32, v138, v139, v32\n" \
  "s_waitcnt lgkmcnt(1)\n" \
  "v_mfma_f32_16x16x32_bf16 v[136:139], v[116:119], v[248:251], 0\n" \
  "v_max3_f32 v35, v124, v125, v35\n" \
  "v_max3_f32 v32, v126, v127, v32\n" \
  "s_waitcnt lgkmcnt(0)\n" \
  "v_mfma_f32_16x16x32_bf16 v[124:127], v[120:123], v[248:251], v[164:167]\n" \
  "v_max3_f32 v35, v128, v129, v35\n" \
  "v_max3_f32 v32, v130, v131, v32\n" \
  "C_nv:\n" \
  "s_waitcnt vmcnt(1)\n" \
  "v_max3_u32 v34, v141, v143, v145\n" \
  "v_max_u32 v34, v34, v147\n" \
  "v_cmp_eq_u32 vcc, s67, v34\n" \
  "s_nop 0\n" \
  "s_cbranch_vccnz F_nv\n" \
  "K_nv:\n" \
  "v_max3_f32 v35, v132, v133, v35\n" \
  "v_max3_f32 v32, v134, v135, v32\n" \
  "ds_write_b64 v5, v[140:141] offset:8192\n" \
  "v_max3_f32 v35, v136, v137, v35\n" \
  "v_max3_f32 v32, v138, v139, v32\n" \
  "ds_write_b64 v5, v[142:143] offset:9216\n" \
  "v_max3_f32 v35, v124, v125, v35\n" \
  "v_max3_f32 v32, v126, v127, v32\n" \
  "ds_write_b64 v5, v[144:145]\n" \
  "v_max_f32 v35, v35, v32\n" \
  "ds_write_b64 v5, v[146:147] offset:1024\n" \
  "v_mov_b32 v32, v35\n" \
  "s_nop 1\n" \
  "v_permlane16_swap_b32 v35, v32\n" \
  "v_max_f32 v35, v35, v32\n" \
  "v_mov_b32 v32, v35\n" \
  "s_nop 1\n" \
  "v_permlane32_swap_b32 v35, v32\n" \
  "v_max_f32 v35, v35, v32\n" \
  "v_sub_f32 v156, v156, v35\n" \
  "v_sub_f32 v157, v157, v35\n" \
  "v_sub_f32 v158, v158, v35\n" \
  "v_sub_f32 v159, v159, v35\n" \
  "v_sub_f32 v160, 0, v35\n" \
  "v_sub_f32 v161, 0, v35\n" \
  "v_sub_f32 v162, 0, v35\n" \
  "v_sub_f32 v163, 0, v35\n" \
  "s_bitcmp1_b32 %[w], 0\n" \
  "s_cbranch_scc1 ODD\n" \
  "v_mfma_f32_16x16x32_bf16 v[124:127], v[56:59], v[248:251], v[156:159]\n" \
  "v_mfma_f32_16x16x32_bf16 v[128:131], v[60:63], v[248:251], v[160:163]\n" \
  "v_mfma_f32_16x16x32_bf16 v[132:135], v[64:67], v[248:251], v[160:163]\n" \
  "v_mfma_f32_16x16x32_bf16 v[136:139], v[68:71], v[248:251], v[160:163]\n" \
  "v_sub_f32 v164, v164, v35\n" \
  "v_sub_f32 v165, v165, v35\n" \
  "v_sub_f32 v166, v166, v35\n" \
  "v_sub_f32 v167, v167, v35\n" \
  "s_waitcnt lgkmcnt(0)\n" \
  "s_barrier\n" \
  "ds_read_b128 v[224:227], v4\n" \
  "ds_read_b128 v[228:231], v4 offset:1024\n" \
  "ds_read_b128 v[232:235], v4 offset:2048\n" \
  "ds_read_b128 v[236:239], v4 offset:3072\n" \
  "v_exp_f32 v124, v124\n" \
  "v_exp_f32 v125, v125\n" \
  "v_exp_f32 v126, v126\n" \
  "v_exp_f32 v127, v127\n" \
  "v_cvt_pk_bf16_f32 v140, v124, v125\n" \
  "v_cvt_pk_bf16_f32 v141, v126, v127\n" \
  "v_exp_f32 v128, v128\n" \
  "v_exp_f32 v129, v129\n" \
  "v_exp_f32 v130, v130\n" \
  "v_exp_f32 v131, v131\n" \
  "v_cvt_pk_bf16_f32 v142, v128, v129\n" \
  "v_cvt_pk_bf16_f32 v143, v130, v131\n" \
  "v_mfma_f32_16x16x32_bf16 v[124:127], v[72:75], v[248:251], v[160:163]\n" \
  "ds_read_b128 v[240:243], v4 offset:4096\n" \
  "s_add_u32 s58, s39, 0\n" \
  "buffer_load_dwordx4 v[56:59], v0, s[28:31], s58 offen sc1\n" \
  "v_exp_f32 v132, v132\n" \
  "v_exp_f32 v133, v133\n" \
  "v_exp_f32 v134, v134\n" \
  "v_mfma_f32_16x16x32_bf16 v[128:131], v[76:79], v[248:251], v[160:163]\n" \
  "ds_read_b128 v[244:247], v4 offset:5120\n" \
  "buffer_load_dwordx4 v[60:63], v0, s[28:31], s58 offen offset:1024 sc1\n" \
  "v_exp_f32 v135, v135\n" \
  "v_cvt_pk_bf16_f32 v144, v132, v133\n" \
  "v_cvt_pk_bf16_f32 v145, v134, v135\n" \
  "s_waitcnt lgkmcnt(4)\n" \
  "v_mfma_f32_16x16x32_bf16 v[152:155], v[228:231], v[140:143], 0\n" \
  "v_exp_f32 v136, v136\n" \
  "v_exp_f32 v137, v137\n" \
  "v_exp_f32 v138, v138\n" \
  "v_mfma_f32_16x16x32_bf16 v[148:151], v[224:227], v[140:143], 0\n" \
  "v_exp_f32 v139, v139\n" \
  "v_cvt_pk_bf16_f32 v146, v136, v137\n" \
  "v_cvt_pk_bf16_f32 v147, v138, v139\n" \
  "v_mfma_f32_16x16x32_bf16 v[132:135], v[80:83], v[248:251], v[160:163]\n" \
  "ds_read_b128 v[224:227], v4 offset:6144\n" \
  "buffer_load_dwordx4 v[64:67], v0, s[28:31], s58 offen offset:2048 sc1\n" \
  "v_exp_f32 v124, v124\n" \
  "v_exp_f32 v125, v125\n" \
  "v_exp_f32 v126, v126\n" \
  "v_mfma_f32_16x16x32_bf16 v[136:139], v[84:87], v[248:251], v[160:163]\n" \
  "ds_read_b128 v[228:231], v4 offset:7168\n" \
  "buffer_load_dwordx4 v[68:71], v0, s[28:31], s58 offen offset:3072 sc1\n" \
  "v_exp_f32 v127, v127\n" \
  "v_cvt_pk_bf16_f32 v140, v124, v125\n" \
  "v_cvt_pk_bf16_f32 v141, v126, v127\n" \
  "s_waitcnt lgkmcnt(4)\n" \
  "v_mfma_f32_16x16x32_bf16 v[152:155], v[236:239], v[144:147], v[152:155]\n" \
  "v_exp_f32 v128, v128\n" \
  "v_exp_f32 v129, v129\n" \
  "v_exp_f32 v130, v130\n" \
  "v_mfma_f32_16x16x32_bf16 v[148:151], v[232:235], v[144:147], v[148:151]\n" \
  "v_exp_f32 v131, v131\n" \
  "v_cvt_pk_bf16_f32 v142, v128, v129\n" \
  "v_cvt_pk_bf16_f32 v143, v130, v131\n" \
  "v_mfma_f32_16x16x32_bf16 v[124:127], v[88:91], v[248:251], v[160:163]\n" \
  "ds_read_b128 v[232:235], v4 offset:8192\n" \
  "s_add_u32 s58, s39, 0x1000\n" \
  "buffer_load_dwordx4 v[72:75], v0, s[28:31], s58 offen sc1\n" \
  "v_exp_f32 v132, v132\n" \
  "v_exp_f32 v133, v133\n" \
  "v_exp_f32 v134, v134\n" \
  "v_mfma_f32_16x16x32_bf16 v[128:131], v[92:95], v[248:251], v[160:163]\n" \
  "ds_read_b128 v[236:239], v4 offset:9216\n" \
  "buffer_load_dwordx4 v[76:79], v0, s[28:31], s58 offen offset:1024 sc1\n" \
  "v_exp_f32 v135, v135\n" \
  "v_cvt_pk_bf16_f32 v144, v132, v133\n" \
  "v_cvt_pk_bf16_f32 v145, v134, v135\n" \
  "s_waitcnt lgkmcnt(4)\n" \
  "v_mfma_f32_16x16x32_bf16 v[152:155], v[244:247], v[140:143], v[152:155]\n" \
  "v_exp_f32 v136, v136\n" \
  "v_exp_f32 v137, v137\n" \
  "v_exp_f32 v138, v138\n" \
  "v_mfma_f32_16x16x32_bf16 v[148:151], v[240:243], v[140:143], v[148:151]\n" \
  "v_exp_f32 v139, v139\n" \
  "v_cvt_pk_bf16_f32 v146, v136, v137\n" \
  "v_cvt_pk_bf16_f32 v147, v138, v139\n" \
  "v_mfma_f32_16x16x32_bf16 v[132:135], v[96:99], v[248:251], v[160:163]\n" \
  "ds_read_b128 v[240:243], v4 offset:10240\n" \
  "buffer_load_dwordx4 v[80:83], v0, s[28:31], s58 offen offset:2048 sc1\n" \
  "v_exp_f32 v124, v124\n" \
  "v_exp_f32 v125, v125\n" \
  "v_exp_f32 v126, v126\n" \
  "v_mfma_f32_16x16x32_bf16 v[136:139], v[100:103], v[248:251], v[160:163]\n" \
  "ds_read_b128 v[244:247], v4 offset:11264\n" \
  "buffer_load_dwordx4 v[84:87], v0, s[28:31], s58 offen offset:3072 sc1\n" \
  "v_exp_f32 v127, v127\n" \
  "v_cvt_pk_bf16_f32 v140, v124, v125\n" \
  "v_cvt_pk_bf16_f32 v141, v126, v127\n" \
  "s_waitcnt lgkmcnt(4)\n" \
  "v_mfma_f32_16x16x32_bf16 v[152:155], v[228:231], v[144:147], v[152:155]\n" \
  "v_exp_f32 v128, v128\n" \
  "v_exp_f32 v129, v129\n" \
  "v_exp_f32 v130, v130\n" \
  "v_mfma_f32_16x16x32_bf16 v[148:151], v[224:227], v[144:147], v[148:151]\n" \
  "v_exp_f32 v131, v131\n" \
  "v_cvt_pk_bf16_f32 v142, v128, v129\n" \
  "v_cvt_pk_bf16_f32 v143, v130, v131\n" \
  "v_mfma_f32_16x16x32_bf16 v[124:127], v[104:107], v[248:251], v[160:163]\n" \
  "ds_read_b128 v[224:227], v4 offset:12288\n" \
  "s_add_u32 s58, s39, 0x2000\n" \
  "buffer_load_dwordx4 v[88:91], v0, s[28:31], s58 offen sc1\n" \
  "v_exp_f32 v132, v132\n" \
  "v_exp_f32 v133, v133\n" \
  "v_exp_f32 v134, v134\n" \
  "v_mfma_f32_16x16x32_bf16 v[128:131], v[108:111], v[248:251], v[160:163]\n" \
  "ds_read_b128 v[228:231], v4 offset:13312\n" \
  "buffer_load_dwordx4 v[92:95], v0, s[28:31], s58 offen offset:1024 sc1\n" \
  "v_exp_f32 v135, v135\n" \
  "v_cvt_pk_bf16_f32 v144, v132, v133\n" \
  "v_cvt_pk_bf16_f32 v145, v134, v135\n" \
  "s_waitcnt lgkmcnt(4)\n" \
  "v_mfma_f32_16x16x32_bf16 v[152:155], v[236:239], v[140:143], v[152:155]\n" \
  "v_exp_f32 v136, v136\n" \
  "v_exp_f32 v137, v137\n" \
  "v_exp_f32 v138, v138\n" \
  "v_mfma_f32_16x16x32_bf16 v[148:151], v[232:235], v[140:143], v[148:151]\n" \
  "v_exp_f32 v139, v139\n" \
  "v_cvt_pk_bf16_f32 v146, v136, v137\n" \
  "v_cvt_pk_bf16_f32 v147, v138, v139\n" \
  "v_mfma_f32_16x16x32_bf16 v[132:135], v[112:115], v[248:251], v[160:163]\n" \
  "ds_read_b128 v[232:235], v4 offset:14336\n" \
  "buffer_load_dwordx4 v[96:99], v0, s[28:31], s58 offen offset:2048 sc1\n" \
  "v_exp_f32 v124, v124\n" \
  "v_exp_f32 v125, v125\n" \
  "v_exp_f32 v126, v126\n" \
  "v_mfma_f32_16x16x32_bf16 v[136:139], v[116:119], v[248:251], v[160:163]\n" \
  "ds_read_b128 v[236:239], v4 offset:15360\n" \
  "buffer_load_dwordx4 v[100:103], v0, s[28:31], s58 offen offset:3072 sc1\n" \
  "v_exp_f32 v127, v127\n" \
  "v_cvt_pk_bf16_f32 v140, v124, v125\n" \
  "v_cvt_pk_bf16_f32 v141, v126, v127\n" \
  "s_waitcnt lgkmcnt(4)\n" \
  "v_mfma_f32_16x16x32_bf16 v[152:155], v[244:247], v[144:147], v[152:155]\n" \
  "v_exp_f32 v128, v128\n" \
  "v_exp_f32 v129, v129\n" \
  "v_exp_f32 v130, v130\n" \
  "v_mfma_f32_16x16x32_bf16 v[148:151], v[240:243], v[144:147], v[148:151]\n" \
  "v_exp_f32 v131, v131\n" \
  "v_cvt_pk_bf16_f32 v142, v128, v129\n" \
  "v_cvt_pk_bf16_f32 v143, v130, v131\n" \
  "v_mfma_f32_16x16x32_bf16 v[124:127], v[120:123], v[248:251], v[164:167]\n" \
  "ds_read_b128 v[240:243], v4 offset:16384\n" \
  "s_add_u32 s58, s39, 0x3000\n" \
  "buffer_load_dwordx4 v[104:107], v0, s[28:31], s58 offen sc1\n" \
  "v_exp_f32 v132, v132\n" \
  "v_exp_f32 v133, v133\n" \
  "v_exp_f32 v134, v134\n" \
  "s_waitcnt lgkmcnt(3)\n" \
  "v_mfma_f32_16x16x32_bf16 v[152:155], v[228:231], v[140:143], v[152:155]\n" \
  "ds_read_b128 v[244:247], v4 offset:17408\n" \
  "buffer_load_dwordx4 v[108:111], v0, s[28:31], s58 offen offset:1024 sc1\n" \
  "v_exp_f32 v135, v135\n" \
  "v_cvt_pk_bf16_f32 v144, v132, v133\n" \
  "v_cvt_pk_bf16_f32 v145, v134, v135\n" \
  "v_mfma_f32_16x16x32_bf16 v[148:151], v[224:227], v[140:143], v[148:151]\n" \
  "v_exp_f32 v136, v136\n" \
  "v_exp_f32 v137, v137\n" \
  "v_exp_f32 v138, v138\n" \
  "v_exp_f32 v139, v139\n" \
  "v_cvt_pk_bf16_f32 v146, v136, v137\n" \
  "v_cvt_pk_bf16_f32 v147, v138, v139\n" \
  "s_waitcnt lgkmcnt(2)\n" \
  "s_nop 0\n" \
  "v_mfma_f32_16x16x32_bf16 v[152:155], v[236:239], v[144:147], v[152:155]\n" \
  "buffer_load_dwordx4 v[112:115], v0, s[28:31], s58 offen offset:2048 sc1\n" \
  "v_exp_f32 v124, v124\n" \
  "v_exp_f32 v125, v125\n" \
  "v_exp_f32 v126, v126\n" \
  "v_mfma_f32_16x16x32_bf16 v[148:151], v[232:235], v[144:147], v[148:151]\n" \
  "buffer_load_dwordx4 v[116:119], v0, s[28:31], s58 offen offset:3072 sc1\n" \
  "v_exp_f32 v127, v127\n" \
  "v_cvt_pk_bf16_f32 v140, v124, v125\n" \
  "v_cvt_pk_bf16_f32 v141, v126, v127\n" \
  "v_mov_b32 v142, 0\n" \
  "v_mov_b32 v143, 0\n" \
  "s_waitcnt lgkmcnt(0)\n" \
  "s_nop 0\n" \
  "v_mfma_f32_16x16x32_bf16 v[152:155], v[244:247], v[140:143], v[152:155]\n" \
  "v_mfma_f32_16x16x32_bf16 v[148:151], v[240:243], v[140:143], v[148:151]\n" \
  "s_branch PVJ\n" \
  "ODD:\n" \
  "s_cmp_eq_u32 %[w], 7\n" \
  "s_cbranch_scc0 ODD1\n" \
  "v_mfma_f32_16x16x32_bf16 v[128:131], v[56:59], v[248:251], v[156:159]\n" \
  "v_mfma_f32_16x16x32_bf16 v[132:135], v[60:63], v[248:251], v[160:163]\n" \
  "v_mfma_f32_16x16x32_bf16 v[136:139], v[64:67], v[248:251], v[160:163]\n" \
  "v_sub_f32 v164, v164, v35\n" \
  "v_sub_f32 v165, v165, v35\n" \
  "v_sub_f32 v166, v166, v35\n" \
  "v_sub_f32 v167, v167, v35\n" \
  "s_waitcnt lgkmcnt(0)\n" \
  "s_barrier\n" \
  "ds_read_b128 v[224:227], v4\n" \
  "ds_read_b128 v[228:231], v4 offset:1024\n" \
  "ds_read_b128 v[232:235], v4 offset:2048\n" \
  "ds_read_b128 v[236:239], v4 offset:3072\n" \
  "v_mov_b32 v140, 0\n" \
  "v_mov_b32 v141, 0\n" \
  "v_exp_f32 v128, v128\n" \
  "v_exp_f32 v129, v129\n" \
  "v_exp_f32 v130, v130\n" \
  "v_exp_f32 v131, v131\n" \
  "v_cvt_pk_bf16_f32 v142, v128, v129\n" \
  "v_cvt_pk_bf16_f32 v143, v130, v131\n" \
  "v_mfma_f32_16x16x32_bf16 v[124:127], v[68:71], v[248:251], v[160:163]\n" \
  "ds_read_b128 v[240:243], v4 offset:4096\n" \
  "v_exp_f32 v132, v132\n" \
  "v_exp_f32 v133, v133\n" \
  "v_exp_f32 v134, v134\n" \
  "v_mfma_f32_16x16x32_bf16 v[128:131], v[72:75], v[248:251], v[160:163]\n" \
  "ds_read_b128 v[244:247], v4 offset:5120\n" \
  "v_exp_f32 v135, v135\n" \
  "v_cvt_pk_bf16_f32 v144, v132, v133\n" \
  "v_cvt_pk_bf16_f32 v145, v134, v135\n" \
  "s_waitcnt lgkmcnt(4)\n" \
  "v_mfma_f32_16x16x32_bf16 v[152:155], v[228:231], v[140:143], 0\n" \
  "v_exp_f32 v136, v136\n" \
  "v_exp_f32 v137, v137\n" \
  "v_exp_f32 v138, v138\n" \
  "v_mfma_f32_16x16x32_bf16 v[148:151], v[224:227], v[140:143], 0\n" \
  "v_exp_f32 v139, v139\n" \
  "v_cvt_pk_bf16_f32 v146, v136, v137\n" \
  "v_cvt_pk_bf16_f32 v147, v138, v139\n" \
  "v_mfma_f32_16x16x32_bf16 v[132:135], v[76:79], v[248:251], v[160:163]\n" \
  "ds_read_b128 v[224:227], v4 offset:6144\n" \
  "v_exp_f32 v124, v124\n" \
  "v_exp_f32 v125, v125\n" \
  "v_exp_f32 v126, v126\n" \
  "v_mfma_f32_16x16x32_bf16 v[136:139], v[80:83], v[248:251], v[160:163]\n" \
  "ds_read_b128 v[228:231], v4 offset:7168\n" \
  "v_exp_f32 v127, v127\n" \
  "v_cvt_pk_bf16_f32 v140, v124, v125\n" \
  "v_cvt_pk_bf16_f32 v141, v126, v127\n" \
  "s_waitcnt lgkmcnt(4)\n" \
  "v_mfma_f32_16x16x32_bf16 v[152:155], v[236:239], v[144:147], v[152:155]\n" \
  "v_exp_f32 v128, v128\n" \
  "v_exp_f32 v129, v129\n" \
  "v_exp_f32 v130, v130\n" \
  "v_mfma_f32_16x16x32_bf16 v[148:151], v[232:235], v[144:147], v[148:151]\n" \
  "v_exp_f32 v131, v131\n" \
  "v_cvt_pk_bf16_f32 v142, v128, v129\n" \
  "v_cvt_pk_bf16_f32 v143, v130, v131\n" \
  "v_mfma_f32_16x16x32_bf16 v[124:127], v[84:87], v[248:251], v[160:163]\n" \
  "ds_read_b128 v[232:235], v4 offset:8192\n" \
  "v_exp_f32 v132, v132\n" \
  "v_exp_f32 v133, v133\n" \
  "v_exp_f32 v134, v134\n" \
  "v_mfma_f32_16x16x32_bf16 v[128:131], v[88:91], v[248:251], v[160:163]\n" \
  "ds_read_b128 v[236:239], v4 offset:9216\n" \
  "v_exp_f32 v135, v135\n" \
  "v_cvt_pk_bf16_f32 v144, v132, v133\n" \
  "v_cvt_pk_bf16_f32 v145, v134, v135\n" \
  "s_waitcnt lgkmcnt(4)\n" \
  "v_mfma_f32_16x16x32_bf16 v[152:155], v[244:247], v[140:143], v[152:155]\n" \
  "v_exp_f32 v136, v136\n" \
  "v_exp_f32 v137, v137\n" \
  "v_exp_f32 v138, v138\n" \
  "v_mfma_f32_16x16x32_bf16 v[148:151], v[240:243], v[140:143], v[148:151]\n" \
  "v_exp_f32 v139, v139\n" \
  "v_cvt_pk_bf16_f32 v146, v136, v137\n" \
  "v_cvt_pk_bf16_f32 v147, v138, v139\n" \
  "v_mfma_f32_16x16x32_bf16 v[132:135], v[92:95], v[248:251], v[160:163]\n" \
  "ds_read_b128 v[240:243], v4 offset:10240\n" \
  "v_exp_f32 v124, v124\n" \
  "v_exp_f32 v125, v125\n" \
  "v_exp_f32 v126, v126\n" \
  "v_mfma_f32_16x16x32_bf16 v[136:139], v[96:99], v[248:251], v[160:163]\n" \
  "ds_read_b128 v[244:247], v4 offset:11264\n" \
  "v_exp_f32 v127, v127\n" \
  "v_cvt_pk_bf16_f32 v140, v124, v125\n" \
  "v_cvt_pk_bf16_f32 v141, v126, v127\n" \
  "s_waitcnt lgkmcnt(4)\n" \
  "v_mfma_f32_16x16x32_bf16 v[152:155], v[228:231], v[144:147], v[152:155]\n" \
  "v_exp_f32 v128, v128\n" \
  "v_exp_f32 v129, v129\n" \
  "v_exp_f32 v130, v130\n" \
  "v_mfma_f32_16x16x32_bf16 v[148:151], v[224:227], v[144:147], v[148:151]\n" \
  "v_exp_f32 v131, v131\n" \
  "v_cvt_pk_bf16_f32 v142, v128, v129\n" \
  "v_cvt_pk_bf16_f32 v143, v130, v131\n" \
  "v_mfma_f32_16x16x32_bf16 v[124:127], v[100:103], v[248:251], v[160:163]\n" \
  "ds_read_b128 v[224:227], v4 offset:12288\n" \
  "v_exp_f32 v132, v132\n" \
  "v_exp_f32 v133, v133\n" \
  "v_exp_f32 v134, v134\n" \
  "v_mfma_f32_16x16x32_bf16 v[128:131], v[104:107], v[248:251], v[160:163]\n" \
  "ds_read_b128 v[228:231], v4 offset:13312\n" \
  "v_exp_f32 v135, v135\n" \
  "v_cvt_pk_bf16_f32 v144, v132, v133\n" \
  "v_cvt_pk_bf16_f32 v145, v134, v135\n" \
  "s_waitcnt lgkmcnt(4)\n" \
  "v_mfma_f32_16x16x32_bf16 v[152:155], v[236:239], v[140:143], v[152:155]\n" \
  "v_exp_f32 v136, v136\n" \
  "v_exp_f32 v137, v137\n" \
  "v_exp_f32 v138, v138\n" \
  "v_mfma_f32_16x16x32_bf16 v[148:151], v[232:235], v[140:143], v[148:151]\n" \
  "v_exp_f32 v139, v139\n" \
  "v_cvt_pk_bf16_f32 v146, v136, v137\n" \
  "v_cvt_pk_bf16_f32 v147, v138, v139\n" \
  "v_mfma_f32_16x16x32_bf16 v[132:135], v[108:111], v[248:251], v[160:163]\n" \
  "ds_read_b128 v[232:235], v4 offset:14336\n" \
  "v_exp_f32 v124, v124\n" \
  "v_exp_f32 v125, v125\n" \
  "v_exp_f32 v126, v126\n" \
  "v_mfma_f32_16x16x32_bf16 v[136:139], v[112:115], v[248:251], v[160:163]\n" \
  "ds_read_b128 v[236:239], v4 offset:15360\n" \
  "v_exp_f32 v127, v127\n" \
  "v_cvt_pk_bf16_f32 v140, v124, v125\n" \
  "v_cvt_pk_bf16_f32 v141, v126, v127\n" \
  "s_waitcnt lgkmcnt(4)\n" \
  "v_mfma_f32_16x16x32_bf16 v[152:155], v[244:247], v[144:147], v[152:155]\n" \
  "v_exp_f32 v128, v128\n" \
  "v_exp_f32 v129, v129\n" \
  "v_exp_f32 v130, v130\n" \
  "v_mfma_f32_16x16x32_bf16 v[148:151], v[240:243], v[144:147], v[148:151]\n" \
  "v_exp_f32 v131, v131\n" \
  "v_cvt_pk_bf16_f32 v142, v128, v129\n" \
  "v_cvt_pk_bf16_f32 v143, v130, v131\n" \
  "v_mfma_f32_16x16x32_bf16 v[124:127], v[116:119], v[248:251], v[160:163]\n" \
  "ds_read_b128 v[240:243], v4 offset:16384\n" \
  "v_exp_f32 v132, v132\n" \
  "v_exp_f32 v133, v133\n" \
  "v_exp_f32 v134, v134\n" \
  "v_mfma_f32_16x16x32_bf16 v[128:131], v[120:123], v[248:251], v[164:167]\n" \
  "ds_read_b128 v[244:247], v4 offset:17408\n" \
  "v_exp_f32 v135, v135\n" \
  "v_cvt_pk_bf16_f32 v144, v132, v133\n" \
  "v_cvt_pk_bf16_f32 v145, v134, v135\n" \
  "s_waitcnt lgkmcnt(4)\n" \
  "v_mfma_f32_16x16x32_bf16 v[152:155], v[228:231], v[140:143], v[152:155]\n" \
  "v_exp_f32 v136, v136\n" \
  "v_exp_f32 v137, v137\n" \
  "v_exp_f32 v138, v138\n" \
  "v_mfma_f32_16x16x32_bf16 v[148:151], v[224:227], v[140:143], v[148:151]\n" \
  "v_exp_f32 v139, v139\n" \
  "v_cvt_pk_bf16_f32 v146, v136, v137\n" \
  "v_cvt_pk_bf16_f32 v147, v138, v139\n" \
  "s_waitcnt lgkmcnt(2)\n" \
  "s_nop 0\n" \
  "v_mfma_f32_16x16x32_bf16 v[152:155], v[236:239], v[144:147], v[152:155]\n" \
  "v_exp_f32 v124, v124\n" \
  "v_exp_f32 v125, v125\n" \
  "v_exp_f32 v126, v126\n" \
  "v_mfma_f32_16x16x32_bf16 v[148:151], v[232:235], v[144:147], v[148:151]\n" \
  "v_exp_f32 v127, v127\n" \
  "v_cvt_pk_bf16_f32 v140, v124, v125\n" \
  "v_cvt_pk_bf16_f32 v141, v126, v127\n" \
  "v_exp_f32 v128, v128\n" \
  "v_exp_f32 v129, v129\n" \
  "v_exp_f32 v130, v130\n" \
  "v_exp_f32 v131, v131\n" \
  "v_cvt_pk_bf16_f32 v142, v128, v129\n" \
  "v_cvt_pk_bf16_f32 v143, v130, v131\n" \
  "s_waitcnt lgkmcnt(0)\n" \
  "s_nop 0\n" \
  "v_mfma_f32_16x16x32_bf16 v[152:155], v[244:247], v[140:143], v[152:155]\n" \
  "v_mfma_f32_16x16x32_bf16 v[148:151], v[240:243], v[140:143], v[148:151]\n" \
  "s_branch PVJ\n" \
  "ODD1:\n" \
  "v_mfma_f32_16x16x32_bf16 v[128:131], v[56:59], v[248:251], v[156:159]\n" \
  "v_mfma_f32_16x16x32_bf16 v[132:135], v[60:63], v[248:251], v[160:163]\n" \
  "v_mfma_f32_16x16x32_bf16 v[136:139], v[64:67], v[248:251], v[160:163]\n" \
  "v_sub_f32 v164, v164, v35\n" \
  "v_sub_f32 v165, v165, v35\n" \
  "v_sub_f32 v166, v166, v35\n" \
  "v_sub_f32 v167, v167, v35\n" \
  "s_waitcnt lgkmcnt(0)\n" \
  "s_barrier\n" \
  "ds_read_b128 v[224:227], v4\n" \
  "ds_read_b128 v[228:231], v4 offset:1024\n" \
  "ds_read_b128 v[232:235], v4 offset:2048\n" \
  "ds_read_b128 v[236:239], v4 offset:3072\n" \
  "v_mov_b32 v140, 0\n" \
  "v_mov_b32 v141, 0\n" \
  "v_exp_f32 v128, v128\n" \
  "v_exp_f32 v129, v129\n" \
  "v_exp_f32 v130, v130\n" \
  "v_exp_f32 v131, v131\n" \
  "v_cvt_pk_bf16_f32 v142, v128, v129\n" \
  "v_cvt_pk_bf16_f32 v143, v130, v131\n" \
  "v_mfma_f32_16x16x32_bf16 v[124:127], v[68:71], v[248:251], v[160:163]\n" \
  "ds_read_b128 v[240:243], v4 offset:4096\n" \
  "s_add_u32 s58, s39, 0\n" \
  "buffer_load_dwordx4 v[56:59], v0, s[28:31], s58 offen sc1\n" \
  "v_exp_f32 v132, v132\n" \
  "v_exp_f32 v133, v133\n" \
  "v_exp_f32 v134, v134\n" \
  "v_mfma_f32_16x16x32_bf16 v[128:131], v[72:75], v[248:251], v[160:163]\n" \
  "ds_read_b128 v[244:247], v4 offset:5120\n" \
  "buffer_load_dwordx4 v[60:63], v0, s[28:31], s58 offen offset:1024 sc1\n" \
  "v_exp_f32 v135, v135\n" \
  "v_cvt_pk_bf16_f32 v144, v132, v133\n" \
  "v_cvt_pk_bf16_f32 v145, v134, v135\n" \
  "s_waitcnt lgkmcnt(4)\n" \
  "v_mfma_f32_16x16x32_bf16 v[152:155], v[228:231], v[140:143], 0\n" \
  "v_exp_f32 v136, v136\n" \
  "v_exp_f32 v137, v137\n" \
  "v_exp_f32 v138, v138\n" \
  "v_mfma_f32_16x16x32_bf16 v[148:151], v[224:227], v[140:143], 0\n" \
  "v_exp_f32 v139, v139\n" \
  "v_cvt_pk_bf16_f32 v146, v136, v137\n" \
  "v_cvt_pk_bf16_f32 v147, v138, v139\n" \
  "v_mfma_f32_16x16x32_bf16 v[132:135], v[76:79], v[248:251], v[160:163]\n" \
  "ds_read_b128 v[224:227], v4 offset:6144\n" \
  "buffer_load_dwordx4 v[64:67], v0, s[28:31], s58 offen offset:2048 sc1\n" \
  "v_exp_f32 v124, v124\n" \
  "v_exp_f32 v125, v125\n" \
  "v_exp_f32 v126, v126\n" \
  "v_mfma_f32_16x16x32_bf16 v[136:139], v[80:83], v[248:251], v[160:163]\n" \
  "ds_read_b128 v[228:231], v4 offset:7168\n" \
  "buffer_load_dwordx4 v[68:71], v0, s[28:31], s58 offen offset:3072 sc1\n" \
  "v_exp_f32 v127, v127\n" \
  "v_cvt_pk_bf16_f32 v140, v124, v125\n" \
  "v_cvt_pk_bf16_f32 v141, v126, v127\n" \
  "s_waitcnt lgkmcnt(4)\n" \
  "v_mfma_f32_16x16x32_bf16 v[152:155], v[236:239], v[144:147], v[152:155]\n" \
  "v_exp_f32 v128, v128\n" \
  "v_exp_f32 v129, v129\n" \
  "v_exp_f32 v130, v130\n" \
  "v_mfma_f32_16x16x32_bf16 v[148:151], v[232:235], v[144:147], v[148:151]\n" \
  "v_exp_f32 v131, v131\n" \
  "v_cvt_pk_bf16_f32 v142, v128, v129\n" \
  "v_cvt_pk_bf16_f32 v143, v130, v131\n" \
  "v_mfma_f32_16x16x32_bf16 v[124:127], v[84:87], v[248:251], v[160:163]\n" \
  "ds_read_b128 v[232:235], v4 offset:8192\n" \
  "s_add_u32 s58, s39, 0x1000\n" \
  "buffer_load_dwordx4 v[72:75], v0, s[28:31], s58 offen sc1\n" \
  "v_exp_f32 v132, v132\n" \
  "v_exp_f32 v133, v133\n" \
  "v_exp_f32 v134, v134\n" \
  "v_mfma_f32_16x16x32_bf16 v[128:131], v[88:91], v[248:251], v[160:163]\n" \
  "ds_read_b128 v[236:239], v4 offset:9216\n" \
  "buffer_load_dwordx4 v[76:79], v0, s[28:31], s58 offen offset:1024 sc1\n" \
  "v_exp_f32 v135, v135\n" \
  "v_cvt_pk_bf16_f32 v144, v132, v133\n" \
  "v_cvt_pk_bf16_f32 v145, v134, v135\n" \
  "s_waitcnt lgkmcnt(4)\n" \
  "v_mfma_f32_16x16x32_bf16 v[152:155], v[244:247], v[140:143], v[152:155]\n" \
  "v_exp_f32 v136, v136\n" \
  "v_exp_f32 v137, v137\n" \
  "v_exp_f32 v138, v138\n" \
  "v_mfma_f32_16x16x32_bf16 v[148:151], v[240:243], v[140:143], v[148:151]\n" \
  "v_exp_f32 v139, v139\n" \
  "v_cvt_pk_bf16_f32 v146, v136, v137\n" \
  "v_cvt_pk_bf16_f32 v147, v138, v139\n" \
  "v_mfma_f32_16x16x32_bf16 v[132:135], v[92:95], v[248:251], v[160:163]\n" \
  "ds_read_b128 v[240:243], v4 offset:10240\n" \
  "buffer_load_dwordx4 v[80:83], v0, s[28:31], s58 offen offset:2048 sc1\n" \
  "v_exp_f32 v124, v124\n" \
  "v_exp_f32 v125, v125\n" \
  "v_exp_f32 v126, v126\n" \
  "v_mfma_f32_16x16x32_bf16 v[136:139], v[96:99], v[248:251], v[160:163]\n" \
  "ds_read_b128 v[244:247], v4 offset:11264\n" \
  "buffer_load_dwordx4 v[84:87], v0, s[28:31], s58 offen offset:3072 sc1\n" \
  "v_exp_f32 v127, v127\n" \
  "v_cvt_pk_bf16_f32 v140, v124, v125\n" \
  "v_cvt_pk_bf16_f32 v141, v126, v127\n" \
  "s_waitcnt lgkmcnt(4)\n" \
  "v_mfma_f32_16x16x32_bf16 v[152:155], v[228:231], v[144:147], v[152:155]\n" \
  "v_exp_f32 v128, v128\n" \
  "v_exp_f32 v129, v129\n" \
  "v_exp_f32 v130, v130\n" \
  "v_mfma_f32_16x16x32_bf16 v[148:151], v[224:227], v[144:147], v[148:151]\n" \
  "v_exp_f32 v131, v131\n" \
  "v_cvt_pk_bf16_f32 v142, v128, v129\n" \
  "v_cvt_pk_bf16_f32 v143, v130, v131\n" \
  "v_mfma_f32_16x16x32_bf16 v[124:127], v[100:103], v[248:251], v[160:163]\n" \
  "ds_read_b128 v[224:227], v4 offset:12288\n" \
  "s_add_u32 s58, s39, 0x2000\n" \
  "buffer_load_dwordx4 v[88:91], v0, s[28:31], s58 offen sc1\n" \
  "v_exp_f32 v132, v132\n" \
  "v_exp_f32 v133, v133\n" \
  "v_exp_f32 v134, v134\n" \
  "v_mfma_f32_16x16x32_bf16 v[128:131], v[104:107], v[248:251], v[160:163]\n" \
  "ds_read_b128 v[228:231], v4 offset:13312\n" \
  "buffer_load_dwordx4 v[92:95], v0, s[28:31], s58 offen offset:1024 sc1\n" \
  "v_exp_f32 v135, v135\n" \
  "v_cvt_pk_bf16_f32 v144, v132, v133\n" \
  "v_cvt_pk_bf16_f32 v145, v134, v135\n" \
  "s_waitcnt lgkmcnt(4)\n" \
  "v_mfma_f32_16x16x32_bf16 v[152:155], v[236:239], v[140:143], v[152:155]\n" \
  "v_exp_f32 v136, v136\n" \
  "v_exp_f32 v137, v137\n" \
  "v_exp_f32 v138, v138\n" \
  "v_mfma_f32_16x16x32_bf16 v[148:151], v[232:235], v[140:143], v[148:151]\n" \
  "v_exp_f32 v139, v139\n" \
  "v_cvt_pk_bf16_f32 v146, v136, v137\n" \
  "v_cvt_pk_bf16_f32 v147, v138, v139\n" \
  "v_mfma_f32_16x16x32_bf16 v[132:135], v[108:111], v[248:251], v[160:163]\n" \
  "ds_read_b128 v[232:235], v4 offset:14336\n" \
  "buffer_load_dwordx4 v[96:99], v0, s[28:31], s58 offen offset:2048 sc1\n" \
  "v_exp_f32 v124, v124\n" \
  "v_exp_f32 v125, v125\n" \
  "v_exp_f32 v126, v126\n" \
  "v_mfma_f32_16x16x32_bf16 v[136:139], v[112:115], v[248:251], v[160:163]\n" \
  "ds_read_b128 v[236:239], v4 offset:15360\n" \
  "buffer_load_dwordx4 v[100:103], v0, s[28:31], s58 offen offset:3072 sc1\n" \
  "v_exp_f32 v127, v127\n" \
  "v_cvt_pk_bf16_f32 v140, v124, v125\n" \
  "v_cvt_pk_bf16_f32 v141, v126, v127\n" \
  "s_waitcnt lgkmcnt(4)\n" \
  "v_mfma_f32_16x16x32_bf16 v[152:155], v[244:247], v[144:147], v[152:155]\n" \
  "v_exp_f32 v128, v128\n" \
  "v_exp_f32 v129, v129\n" \
  "v_exp_f32 v130, v130\n" \
  "v_mfma_f32_16x16x32_bf16 v[148:151], v[240:243], v[144:147], v[148:151]\n" \
  "v_exp_f32 v131, v131\n" \
  "v_cvt_pk_bf16_f32 v142, v128, v129\n" \
  "v_cvt_pk_bf16_f32 v143, v130, v131\n" \
  "v_mfma_f32_16x16x32_bf16 v[124:127], v[116:119], v[248:251], v[160:163]\n" \
  "ds_read_b128 v[240:243], v4 offset:16384\n" \
  "s_add_u32 s58, s39, 0x3000\n" \
  "buffer_load_dwordx4 v[104:107], v0, s[28:31], s58 offen sc1\n" \
  "v_exp_f32 v132, v132\n" \
  "v_exp_f32 v133, v133\n" \
  "v_exp_f32 v134, v134\n" \
  "v_mfma_f32_16x16x32_bf16 v[128:131], v[120:123], v[248:251], v[164:167]\n" \
  "ds_read_b128 v[244:247], v4 offset:17408\n" \
  "buffer_load_dwordx4 v[108:111], v0, s[28:31], s58 offen offset:1024 sc1\n" \
  "v_exp_f32 v135, v135\n" \
  "v_cvt_pk_bf16_f32 v144, v132, v133\n" \
  "v_cvt_pk_bf16_f32 v145, v134, v135\n" \
  "s_waitcnt lgkmcnt(4)\n" \
  "v_mfma_f32_16x16x32_bf16 v[152:155], v[228:231], v[140:143], v[152:155]\n" \
  "v_exp_f32 v136, v136\n" \
  "v_exp_f32 v137, v137\n" \
  "v_exp_f32 v138, v138\n" \
  "v_mfma_f32_16x16x32_bf16 v[148:151], v[224:227], v[140:143], v[148:151]\n" \
  "v_exp_f32 v139, v139\n" \
  "v_cvt_pk_bf16_f32 v146, v136, v137\n" \
  "v_cvt_pk_bf16_f32 v147, v138, v139\n" \
  "s_waitcnt lgkmcnt(2)\n" \
  "s_nop 0\n" \
  "v_mfma_f32_16x16x32_bf16 v[152:155], v[236:239], v[144:147], v[152:155]\n" \
  "buffer_load_dwordx4 v[112:115], v0, s[28:31], s58 offen offset:2048 sc1\n" \
  "v_exp_f32 v124, v124\n" \
  "v_exp_f32 v125, v125\n" \
  "v_exp_f32 v126, v126\n" \
  "v_mfma_f32_16x16x32_bf16 v[148:151], v[232:235], v[144:147], v[148:151]\n" \
  "buffer_load_dwordx4 v[116:119], v0, s[28:31], s58 offen offset:3072 sc1\n" \
  "v_exp_f32 v127, v127\n" \
  "v_cvt_pk_bf16_f32 v140, v124, v125\n" \
  "v_cvt_pk_bf16_f32 v141, v126, v127\n" \
  "v_exp_f32 v128, v128\n" \
  "v_exp_f32 v129, v129\n" \
  "v_exp_f32 v130, v130\n" \
  "v_exp_f32 v131, v131\n" \
  "v_cvt_pk_bf16_f32 v142, v128, v129\n" \
  "v_cvt_pk_bf16_f32 v143, v130, v131\n" \
  "s_waitcnt lgkmcnt(0)\n" \
  "s_nop 0\n" \
  "v_mfma_f32_16x16x32_bf16 v[152:155], v[244:247], v[140:143], v[152:155]\n" \
  "v_mfma_f32_16x16x32_bf16 v[148:151], v[240:243], v[140:143], v[148:151]\n" \
  "PVJ:\n" \
  "s_nop 6\n" \
  "v_mov_b32 v34, v152\n" \
  "v_mov_b32 v35, v152\n" \
  "s_nop 1\n" \
  "v_permlane32_swap_b32 v34, v35\n" \
  "v_mov_b32 v34, v35\n" \
  "s_nop 1\n" \
  "v_permlane16_swap_b32 v34, v35\n" \
  "v_rcp_f32 v35, v35\n" \
  "s_nop 0\n" \
  "v_mul_f32 v148, v35, v148\n" \
  "v_mul_f32 v149, v35, v149\n" \
  "v_mul_f32 v150, v35, v150\n" \
  "v_mul_f32 v151, v35, v151\n" \
  "v_cvt_pk_bf16_f32 v124, v148, v149\n" \
  "v_cvt_pk_bf16_f32 v125, v150, v151\n" \
  "v_mul_f32 v152, v35, v152\n" \
  "v_mul_f32 v153, v35, v153\n" \
  "v_mul_f32 v154, v35, v154\n" \
  "v_mul_f32 v155, v35, v155\n" \
  "v_cvt_pk_bf16_f32 v126, v152, v153\n" \
  "v_cvt_pk_bf16_f32 v127, v154, v155\n" \
  "buffer_store_dwordx4 v[124:127], v0, s[28:31], s53 offen sc1\n" \
  "s_setprio 0\n" \
  "ds_read_b64 v[216:217], v24\n" \
  "ds_read_b64 v[218:219], v24 offset:128\n" \
  "s_waitcnt vmcnt(17)\n" \
  "v_mfma_f32_16x16x32_bf16 v[208:211], v[40:43], v[16:19], 0\n" \
  "v_mfma_f32_16x16x32_bf16 v[212:215], v[40:43], v[20:23], 0\n" \
  "v_add_u32 v44, 0x1b000, v0\n" \
  "s_lshl_b32 s58, %[w], 12\n" \
  "v_add_u32 v45, s58, v44\n" \
  "v_lshrrev_b32 v48, 1, v0\n" \
  "s_lshl_b32 s58, %[w], 8\n" \
  "s_add_u32 s58, s58, 0x23800\n" \
  "v_and_b32 v46, 0x78, v48\n" \
  "v_add_u32 v46, s58, v46\n" \
  "v_lshrrev_b32 v34, 9, v0\n" \
  "v_bfe_u32 v35, v0, 8, 1\n" \
  "v_lshl_add_u32 v46, v34, 7, v46\n" \
  "v_lshl_add_u32 v46, v35, 2, v46\n" \
  "v_and_b32 v35, 0x1f0, v0\n" \
  "v_mul_u32_u24 v47, 56, v35\n" \
  "v_lshl_add_u32 v47, v34, 4, v47\n" \
  "v_lshlrev_b32 v128, 16, v36\n" \
  "v_and_b32 v129, 0xffff0000, v36\n" \
  "v_lshlrev_b32 v130, 16, v37\n" \
  "v_and_b32 v131, 0xffff0000, v37\n" \
  "v_lshlrev_b32 v132, 16, v38\n" \
  "v_and_b32 v133, 0xffff0000, v38\n" \
  "v_lshlrev_b32 v134, 16, v39\n" \
  "v_and_b32 v135, 0xffff0000, v39\n" \
  "s_waitcnt lgkmcnt(1)\n" \
  "v_sub_f32 v200, v128, v216\n" \
  "v_sub_f32 v201, v129, v216\n" \
  "v_sub_f32 v202, v130, v216\n" \
  "v_sub_f32 v203, v131, v216\n" \
  "s_waitcnt lgkmcnt(0)\n" \
  "v_sub_f32 v204, v132, v218\n" \
  "v_sub_f32 v205, v133, v218\n" \
  "v_sub_f32 v206, v134, v218\n" \
  "v_sub_f32 v207, v135, v218\n" \
  "v_cvt_pk_bf16_f32 v200, 0, v200\n" \
  "v_cvt_pk_bf16_f32 v201, 0, v201\n" \
  "v_cvt_pk_bf16_f32 v202, 0, v202\n" \
  "v_cvt_pk_bf16_f32 v203, 0, v203\n" \
  "v_cvt_pk_bf16_f32 v204, 0, v204\n" \
  "v_cvt_pk_bf16_f32 v205, 0, v205\n" \
  "v_cvt_pk_bf16_f32 v206, 0, v206\n" \
  "v_cvt_pk_bf16_f32 v207, 0, v207\n" \
  "v_mul_f32 v200, v217, v200\n" \
  "v_mul_f32 v201, v217, v201\n" \
  "v_mul_f32 v202, v217, v202\n" \
  "v_mul_f32 v203, v217, v203\n" \
  "v_mul_f32 v204, v219, v204\n" \
  "v_mul_f32 v205, v219, v205\n" \
  "v_mul_f32 v206, v219, v206\n" \
  "v_mul_f32 v207, v219, v207\n" \
  "v_cvt_pk_bf16_f32 v200, 0, v200\n" \
  "v_cvt_pk_bf16_f32 v201, 0, v201\n" \
  "v_cvt_pk_bf16_f32 v202, 0, v202\n" \
  "v_cvt_pk_bf16_f32 v203, 0, v203\n" \
  "v_cvt_pk_bf16_f32 v204, 0, v204\n" \
  "v_cvt_pk_bf16_f32 v205, 0, v205\n" \
  "v_cvt_pk_bf16_f32 v206, 0, v206\n" \
  "v_cvt_pk_bf16_f32 v207, 0, v207\n" \
  "v_mul_f32 v200, v208, v200\n" \
  "v_mul_f32 v201, v209, v201\n" \
  "v_mul_f32 v202, v210, v202\n" \
  "v_mul_f32 v203, v211, v203\n" \
  "v_mul_f32 v204, v212, v204\n" \
  "v_mul_f32 v205, v213, v205\n" \
  "v_mul_f32 v206, v214, v206\n" \
  "v_mul_f32 v207, v215, v207\n" \
  "v_cvt_pk_bf16_f32 v200, 0, v200\n" \
  "v_cvt_pk_bf16_f32 v201, 0, v201\n" \
  "v_cvt_pk_bf16_f32 v202, 0, v202\n" \
  "v_cvt_pk_bf16_f32 v203, 0, v203\n" \
  "v_cvt_pk_bf16_f32 v204, 0, v204\n" \
  "v_cvt_pk_bf16_f32 v205, 0, v205\n" \
  "v_cvt_pk_bf16_f32 v206, 0, v206\n" \
  "v_cvt_pk_bf16_f32 v207, 0, v207\n" \
  "v_add_f32 v128, v128, v200\n" \
  "v_add_f32 v129, v129, v201\n" \
  "v_add_f32 v130, v130, v202\n" \
  "v_add_f32 v131, v131, v203\n" \
  "v_add_f32 v132, v132, v204\n" \
  "v_add_f32 v133, v133, v205\n" \
  "v_add_f32 v134, v134, v206\n" \
  "v_add_f32 v135, v135, v207\n" \
  "v_cvt_pk_bf16_f32 v128, 0, v128\n" \
  "v_cvt_pk_bf16_f32 v129, 0, v129\n" \
  "v_cvt_pk_bf16_f32 v130, 0, v130\n" \
  "v_cvt_pk_bf16_f32 v131, 0, v131\n" \
  "v_cvt_pk_bf16_f32 v132, 0, v132\n" \
  "v_cvt_pk_bf16_f32 v133, 0, v133\n" \
  "v_cvt_pk_bf16_f32 v134, 0, v134\n" \
  "v_cvt_pk_bf16_f32 v135, 0, v135\n" \
  "s_waitcnt vmcnt(0)\n" \
  "s_waitcnt lgkmcnt(0)\n" \
  "s_barrier\n" \
  "buffer_load_dwordx4 v[136:139], v0, s[28:31], s54 offen sc1\n" \
  "buffer_load_dwordx4 v[140:143], v0, s[28:31], s54 offen offset:1024 sc1\n" \
  "buffer_load_dwordx4 v[144:147], v0, s[28:31], s54 offen offset:2048 sc1\n" \
  "buffer_load_dwordx4 v[148:151], v0, s[28:31], s54 offen offset:3072 sc1\n" \
  "C_o:\n" \
  "s_waitcnt vmcnt(2)\n" \
  "v_max3_u32 v34, v137, v139, v141\n" \
  "s_waitcnt vmcnt(1)\n" \
  "v_max3_u32 v34, v34, v143, v145\n" \
  "s_waitcnt vmcnt(0)\n" \
  "v_max3_u32 v34, v34, v147, v149\n" \
  "v_max_u32 v34, v34, v151\n" \
  "v_cmp_eq_u32 vcc, -1, v34\n" \
  "s_nop 0\n" \
  "s_cbranch_vccnz F_o\n" \
  "K_o:\n" \
  "ds_write_b128 v45, v[136:139]\n" \
  "ds_write_b128 v45, v[140:143] offset:1024\n" \
  "ds_write_b128 v45, v[144:147] offset:2048\n" \
  "ds_write_b128 v45, v[148:151] offset:3072\n" \
  "s_waitcnt lgkmcnt(0)\n" \
  "s_barrier\n" \
  "s_cmp_eq_u32 %[w], 7\n" \
  "s_cbranch_scc1 OPJ\n" \
  "ds_read_b128 v[224:227], v44\n" \
  "ds_read_b128 v[228:231], v44 offset:16384\n" \
  "ds_read_b128 v[232:235], v44 offset:1024\n" \
  "ds_read_b128 v[236:239], v44 offset:17408\n" \
  "ds_read_b128 v[240:243], v44 offset:2048\n" \
  "ds_read_b128 v[244:247], v44 offset:18432\n" \
  "ds_read_b128 v[152:155], v44 offset:3072\n" \
  "ds_read_b128 v[156:159], v44 offset:19456\n" \
  "ds_read_b128 v[160:163], v44 offset:4096\n" \
  "ds_read_b128 v[164:167], v44 offset:20480\n" \
  "s_cmp_eq_u32 s68, 2\n" \
  "s_cbranch_scc1 OPY\n" \
  "s_waitcnt lgkmcnt(9)\n" \
  "v_mfma_f32_16x16x32_bf16 v[128:131], v[56:59], v[224:227], v[128:131]\n" \
  "ds_read_b128 v[224:227], v44 offset:5120\n" \
  "s_waitcnt lgkmcnt(9)\n" \
  "v_mfma_f32_16x16x32_bf16 v[132:135], v[56:59], v[228:231], v[132:135]\n" \
  "ds_read_b128 v[228:231], v44 offset:21504\n" \
  "s_waitcnt lgkmcnt(9)\n" \
  "v_mfma_f32_16x16x32_bf16 v[128:131], v[60:63], v[232:235], v[128:131]\n" \
  "ds_read_b128 v[232:235], v44 offset:6144\n" \
  "s_waitcnt lgkmcnt(9)\n" \
  "v_mfma_f32_16x16x32_bf16 v[132:135], v[60:63], v[236:239], v[132:135]\n" \
  "ds_read_b128 v[236:239], v44 offset:22528\n" \
  "s_waitcnt lgkmcnt(9)\n" \
  "v_mfma_f32_16x16x32_bf16 v[128:131], v[64:67], v[240:243], v[128:131]\n" \
  "ds_read_b128 v[240:243], v44 offset:7168\n" \
  "s_waitcnt lgkmcnt(9)\n" \
  "v_mfma_f32_16x16x32_bf16 v[132:135], v[64:67], v[244:247], v[132:135]\n" \
  "ds_read_b128 v[244:247], v44 offset:23552\n" \
  "s_waitcnt lgkmcnt(9)\n" \
  "v_mfma_f32_16x16x32_bf16 v[128:131], v[68:71], v[152:155], v[128:131]\n" \
  "ds_read_b128 v[152:155], v44 offset:8192\n" \
  "s_waitcnt lgkmcnt(9)\n" \
  "v_mfma_f32_16x16x32_bf16 v[132:135], v[68:71], v[156:159], v[132:135]\n" \
  "ds_read_b128 v[156:159], v44 offset:24576\n" \
  "s_waitcnt lgkmcnt(9)\n" \
  "v_mfma_f32_16x16x32_bf16 v[128:131], v[72:75], v[160:163], v[128:131]\n" \
  "ds_read_b128 v[160:163], v44 offset:9216\n" \
  "s_waitcnt lgkmcnt(9)\n" \
  "v_mfma_f32_16x16x32_bf16 v[132:135], v[72:75], v[164:167], v[132:135]\n" \
  "ds_read_b128 v[164:167], v44 offset:25600\n" \
  "s_waitcnt lgkmcnt(9)\n" \
  "v_mfma_f32_16x16x32_bf16 v[128:131], v[76:79], v[224:227], v[128:131]\n" \
  "ds_read_b128 v[224:227], v44 offset:10240\n" \
  "s_waitcnt lgkmcnt(9)\n" \
  "v_mfma_f32_16x16x32_bf16 v[132:135], v[76:79], v[228:231], v[132:135]\n" \
  "ds_read_b128 v[228:231], v44 offset:26624\n" \
  "s_waitcnt lgkmcnt(9)\n" \
  "v_mfma_f32_16x16x32_bf16 v[128:131], v[80:83], v[232:235], v[128:131]\n" \
  "ds_read_b128 v[232:235], v44 offset:11264\n" \
  "s_waitcnt lgkmcnt(9)\n" \
  "v_mfma_f32_16x16x32_bf16 v[132:135], v[80:83], v[236:239], v[132:135]\n" \
  "ds_read_b128 v[236:239], v44 offset:27648\n" \
  "s_waitcnt lgkmcnt(9)\n" \
  "v_mfma_f32_16x16x32_bf16 v[128:131], v[84:87], v[240:243], v[128:131]\n" \
  "ds_read_b128 v[240:243], v44 offset:12288\n" \
  "s_waitcnt lgkmcnt(9)\n" \
  "v_mfma_f32_16x16x32_bf16 v[132:135], v[84:87], v[244:247], v[132:135]\n" \
  "ds_read_b128 v[244:247], v44 offset:28672\n" \
  "s_waitcnt lgkmcnt(9)\n" \
  "v_mfma_f32_16x16x32_bf16 v[128:131], v[88:91], v[152:155], v[128:131]\n" \
  "ds_read_b128 v[152:155], v44 offset:13312\n" \
  "s_waitcnt lgkmcnt(9)\n" \
  "v_mfma_f32_16x16x32_bf16 v[132:135], v[88:91], v[156:159], v[132:135]\n" \
  "ds_read_b128 v[156:159], v44 offset:29696\n" \
  "s_waitcnt lgkmcnt(9)\n" \
  "v_mfma_f32_16x16x32_bf16 v[128:131], v[92:95], v[160:163], v[128:131]\n" \
  "ds_read_b128 v[160:163], v44 offset:14336\n" \
  "s_waitcnt lgkmcnt(9)\n" \
  "v_mfma_f32_16x16x32_bf16 v[132:135], v[92:95], v[164:167], v[132:135]\n" \
  "ds_read_b128 v[164:167], v44 offset:30720\n" \
  "s_waitcnt lgkmcnt(9)\n" \
  "v_mfma_f32_16x16x32_bf16 v[128:131], v[96:99], v[224:227], v[128:131]\n" \
  "ds_read_b128 v[224:227], v44 offset:15360\n" \
  "s_waitcnt lgkmcnt(9)\n" \
  "v_mfma_f32_16x16x32_bf16 v[132:135], v[96:99], v[228:231], v[132:135]\n" \
  "ds_read_b128 v[228:231], v44 offset:31744\n" \
  "s_waitcnt lgkmcnt(9)\n" \
  "v_mfma_f32_16x16x32_bf16 v[128:131], v[100:103], v[232:235], v[128:131]\n" \
  "s_waitcnt lgkmcnt(8)\n" \
  "v_mfma_f32_16x16x32_bf16 v[132:135], v[100:103], v[236:239], v[132:135]\n" \
  "s_waitcnt lgkmcnt(7)\n" \
  "v_mfma_f32_16x16x32_bf16 v[128:131], v[104:107], v[240:243], v[128:131]\n" \
  "s_waitcnt lgkmcnt(6)\n" \
  "v_mfma_f32_16x16x32_bf16 v[132:135], v[104:107], v[244:247], v[132:135]\n" \
  "s_waitcnt lgkmcnt(5)\n" \
  "v_mfma_f32_16x16x32_bf16 v[128:131], v[108:111], v[152:155], v[128:131]\n" \
  "s_waitcnt lgkmcnt(4)\n" \
  "v_mfma_f32_16x16x32_bf16 v[132:135], v[108:111], v[156:159], v[132:135]\n" \
  "s_waitcnt lgkmcnt(3)\n" \
  "v_mfma_f32_16x16x32_bf16 v[128:131], v[112:115], v[160:163], v[128:131]\n" \
  "s_waitcnt lgkmcnt(2)\n" \
  "v_mfma_f32_16x16x32_bf16 v[132:135], v[112:115], v[164:167], v[132:135]\n" \
  "s_waitcnt lgkmcnt(1)\n" \
  "v_mfma_f32_16x16x32_bf16 v[128:131], v[116:119], v[224:227], v[128:131]\n" \
  "s_waitcnt lgkmcnt(0)\n" \
  "v_mfma_f32_16x16x32_bf16 v[132:135], v[116:119], v[228:231], v[132:135]\n" \
  "s_branch OPZ\n" \
  "OPY:\n" \
  "s_waitcnt lgkmcnt(9)\n" \
  "v_mfma_f32_16x16x32_bf16 v[128:131], v[56:59], v[224:227], v[128:131]\n" \
  "ds_read_b128 v[224:227], v44 offset:5120\n" \
  "v_mov_b32 v220, -1\n" \
  "v_mov_b32 v221, -1\n" \
  "v_mov_b32 v222, -1\n" \
  "v_mov_b32 v223, -1\n" \
  "s_add_u32 s58, s53, 0x600000\n" \
  "buffer_store_dwordx4 v[220:223], v0, s[28:31], s58 offen sc1\n" \
  "buffer_load_dwordx4 v[168:171], v1, s[28:31], s46 offen sc1\n" \
  "s_waitcnt lgkmcnt(9)\n" \
  "v_mfma_f32_16x16x32_bf16 v[132:135], v[56:59], v[228:231], v[132:135]\n" \
  "ds_read_b128 v[228:231], v44 offset:21504\n" \
  "s_waitcnt lgkmcnt(9)\n" \
  "v_mfma_f32_16x16x32_bf16 v[128:131], v[60:63], v[232:235], v[128:131]\n" \
  "ds_read_b128 v[232:235], v44 offset:6144\n" \
  "s_waitcnt lgkmcnt(9)\n" \
  "v_mfma_f32_16x16x32_bf16 v[132:135], v[60:63], v[236:239], v[132:135]\n" \
  "ds_read_b128 v[236:239], v44 offset:22528\n" \
  "s_waitcnt lgkmcnt(9)\n" \
  "v_mfma_f32_16x16x32_bf16 v[128:131], v[64:67], v[240:243], v[128:131]\n" \
  "ds_read_b128 v[240:243], v44 offset:7168\n" \
  "s_waitcnt lgkmcnt(9)\n" \
  "v_mfma_f32_16x16x32_bf16 v[132:135], v[64:67], v[244:247], v[132:135]\n" \
  "ds_read_b128 v[244:247], v44 offset:23552\n" \
  "s_waitcnt lgkmcnt(9)\n" \
  "v_mfma_f32_16x16x32_bf16 v[128:131], v[68:71], v[152:155], v[128:131]\n" \
  "ds_read_b128 v[152:155], v44 offset:8192\n" \
  "buffer_load_dwordx4 v[172:175], v1, s[28:31], s46 offen offset:1024 sc1\n" \
  "s_waitcnt lgkmcnt(9)\n" \
  "v_mfma_f32_16x16x32_bf16 v[132:135], v[68:71], v[156:159], v[132:135]\n" \
  "ds_read_b128 v[156:159], v44 offset:24576\n" \
  "s_waitcnt lgkmcnt(9)\n" \
  "v_mfma_f32_16x16x32_bf16 v[128:131], v[72:75], v[160:163], v[128:131]\n" \
  "ds_read_b128 v[160:163], v44 offset:9216\n" \
  "s_waitcnt lgkmcnt(9)\n" \
  "v_mfma_f32_16x16x32_bf16 v[132:135], v[72:75], v[164:167], v[132:135]\n" \
  "ds_read_b128 v[164:167], v44 offset:25600\n" \
  "s_waitcnt lgkmcnt(9)\n" \
  "v_mfma_f32_16x16x32_bf16 v[128:131], v[76:79], v[224:227], v[128:131]\n" \
  "ds_read_b128 v[224:227], v44 offset:10240\n" \
  "s_waitcnt lgkmcnt(9)\n" \
  "v_mfma_f32_16x16x32_bf16 v[132:135], v[76:79], v[228:231], v[132:135]\n" \
  "ds_read_b128 v[228:231], v44 offset:26624\n" \
  "s_waitcnt lgkmcnt(9)\n" \
  "v_mfma_f32_16x16x32_bf16 v[128:131], v[80:83], v[232:235], v[128:131]\n" \
  "ds_read_b128 v[232:235], v44 offset:11264\n" \
  "buffer_load_dwordx4 v[176:179], v1, s[28:31], s46 offen offset:2048 sc1\n" \
  "s_waitcnt lgkmcnt(9)\n" \
  "v_mfma_f32_16x16x32_bf16 v[132:135], v[80:83], v[236:239], v[132:135]\n" \
  "ds_read_b128 v[236:239], v44 offset:27648\n" \
  "s_waitcnt lgkmcnt(9)\n" \
  "v_mfma_f32_16x16x32_bf16 v[128:131], v[84:87], v[240:243], v[128:131]\n" \
  "ds_read_b128 v[240:243], v44 offset:12288\n" \
  "s_waitcnt lgkmcnt(9)\n" \
  "v_mfma_f32_16x16x32_bf16 v[132:135], v[84:87], v[244:247], v[132:135]\n" \
  "ds_read_b128 v[244:247], v44 offset:28672\n" \
  "s_waitcnt lgkmcnt(9)\n" \
  "v_mfma_f32_16x16x32_bf16 v[128:131], v[88:91], v[152:155], v[128:131]\n" \
  "ds_read_b128 v[152:155], v44 offset:13312\n" \
  "s_waitcnt lgkmcnt(9)\n" \
  "v_mfma_f32_16x16x32_bf16 v[132:135], v[88:91], v[156:159], v[132:135]\n" \
  "ds_read_b128 v[156:159], v44 offset:29696\n" \
  "s_waitcnt lgkmcnt(9)\n" \
  "v_mfma_f32_16x16x32_bf16 v[128:131], v[92:95], v[160:163], v[128:131]\n" \
  "ds_read_b128 v[160:163], v44 offset:14336\n" \
  "buffer_load_dwordx4 v[180:183], v1, s[28:31], s46 offen offset:3072 sc1\n" \
  "s_waitcnt lgkmcnt(9)\n" \
  "v_mfma_f32_16x16x32_bf16 v[132:135], v[92:95], v[164:167], v[132:135]\n" \
  "ds_read_b128 v[164:167], v44 offset:30720\n" \
  "s_waitcnt lgkmcnt(9)\n" \
  "v_mfma_f32_16x16x32_bf16 v[128:131], v[96:99], v[224:227], v[128:131]\n" \
  "ds_read_b128 v[224:227], v44 offset:15360\n" \
  "s_waitcnt lgkmcnt(9)\n" \
  "v_mfma_f32_16x16x32_bf16 v[132:135], v[96:99], v[228:231], v[132:135]\n" \
  "ds_read_b128 v[228:231], v44 offset:31744\n" \
  "s_waitcnt lgkmcnt(9)\n" \
  "v_mfma_f32_16x16x32_bf16 v[128:131], v[100:103], v[232:235], v[128:131]\n" \
  "s_waitcnt lgkmcnt(8)\n" \
  "v_mfma_f32_16x16x32_bf16 v[132:135], v[100:103], v[236:239], v[132:135]\n" \
  "s_waitcnt lgkmcnt(7)\n" \
  "v_mfma_f32_16x16x32_bf16 v[128:131], v[104:107], v[240:243], v[128:131]\n" \
  "s_waitcnt lgkmcnt(6)\n" \
  "v_mfma_f32_16x16x32_bf16 v[132:135], v[104:107], v[244:247], v[132:135]\n" \
  "s_waitcnt lgkmcnt(5)\n" \
  "v_mfma_f32_16x16x32_bf16 v[128:131], v[108:111], v[152:155], v[128:131]\n" \
  "s_waitcnt lgkmcnt(4)\n" \
  "v_mfma_f32_16x16x32_bf16 v[132:135], v[108:111], v[156:159], v[132:135]\n" \
  "s_waitcnt lgkmcnt(3)\n" \
  "v_mfma_f32_16x16x32_bf16 v[128:131], v[112:115], v[160:163], v[128:131]\n" \
  "s_waitcnt lgkmcnt(2)\n" \
  "v_mfma_f32_16x16x32_bf16 v[132:135], v[112:115], v[164:167], v[132:135]\n" \
  "s_waitcnt lgkmcnt(1)\n" \
  "v_mfma_f32_16x16x32_bf16 v[128:131], v[116:119], v[224:227], v[128:131]\n" \
  "s_waitcnt lgkmcnt(0)\n" \
  "v_mfma_f32_16x16x32_bf16 v[132:135], v[116:119], v[228:231], v[132:135]\n" \
  "OPZ:\n" \
  "s_cmp_eq_u32 s56, 1\n" \
  "s_nop 4\n" \
  "v_cvt_pk_bf16_f32 v124, v128, v129\n" \
  "s_nop 0\n" \
  "v_cvt_pk_bf16_f32 v126, v132, v133\n" \
  "v_cvt_pk_bf16_f32 v125, v130, v131\n" \
  "v_cvt_pk_bf16_f32 v127, v134, v135\n" \
  "v_mov_b32 v36, v124\n" \
  "v_mov_b32 v37, v125\n" \
  "v_mov_b32 v38, v126\n" \
  "v_mov_b32 v39, v127\n" \
  "v_permlane16_swap_b32 v124, v126\n" \
  "v_permlane16_swap_b32 v125, v127\n" \
  "s_cbranch_scc1 FIN\n" \
  "buffer_store_dwordx4 v[124:127], v13, s[28:31], s38 offen sc1\n" \
  "v_add_f32 v140, v128, v129\n" \
  "v_add_f32 v141, v132, v133\n" \
  "v_mul_f32 v142, v128, v128\n" \
  "v_mul_f32 v143, v132, v132\n" \
  "v_fmac_f32 v142, v129, v129\n" \
  "v_fmac_f32 v143, v133, v133\n" \
  "v_add_f32 v140, v140, v130\n" \
  "v_add_f32 v141, v141, v134\n" \
  "v_fmac_f32 v142, v130, v130\n" \
  "v_fmac_f32 v143, v134, v134\n" \
  "v_add_f32 v140, v140, v131\n" \
  "v_add_f32 v141, v141, v135\n" \
  "v_fmac_f32 v142, v131, v131\n" \
  "v_fmac_f32 v143, v135, v135\n" \
  "v_permlane32_swap_b32 v140, v141\n" \
  "s_nop 0\n" \
  "v_permlane32_swap_b32 v142, v143\n" \
  "v_add_f32 v140, v140, v141\n" \
  "v_add_f32 v142, v142, v143\n" \
  "s_nop 1\n" \
  "v_permlane16_swap_b32 v140, v142\n" \
  "v_add_f32 v140, v140, v142\n" \
  "ds_write_b32 v46, v140\n" \
  "s_waitcnt vmcnt(5)\n" \
  "s_waitcnt lgkmcnt(0)\n" \
  "s_barrier\n" \
  "s_branch SPJ\n" \
  "OPJ:\n" \
  "s_cmp_eq_u32 s56, 1\n" \
  "s_cbranch_scc1 FIN7\n" \
  "v_and_b32 v34, 0xf8, v48\n" \
  "v_add_u32 v34, 0x23800, v34\n" \
  "v_and_b32 v32, 0x78, v48\n" \
  "v_and_b32 v35, 0x80, v48\n" \
  "v_lshl_add_u32 v32, v35, 2, v32\n" \
  "v_mov_b32 v220, -1\n" \
  "v_mov_b32 v221, -1\n" \
  "v_mov_b32 v222, -1\n" \
  "v_mov_b32 v223, -1\n" \
  "s_add_u32 s58, s53, 0x600000\n" \
  "buffer_store_dwordx4 v[220:223], v0, s[28:31], s58 offen sc1\n" \
  "buffer_load_dwordx4 v[168:171], v1, s[28:31], s46 offen sc1\n" \
  "buffer_load_dwordx4 v[172:175], v1, s[28:31], s46 offen offset:1024 sc1\n" \
  "buffer_load_dwordx4 v[176:179], v1, s[28:31], s46 offen offset:2048 sc1\n" \
  "buffer_load_dwordx4 v[180:183], v1, s[28:31], s46 offen offset:3072 sc1\n" \
  "s_waitcnt vmcnt(4)\n" \
  "s_waitcnt lgkmcnt(0)\n" \
  "s_barrier\n" \
  "ds_read_b64 v[124:125], v34\n" \
  "ds_read_b64 v[126:127], v34 offset:256\n" \
  "ds_read_b64 v[128:129], v34 offset:512\n" \
  "ds_read_b64 v[130:131], v34 offset:768\n" \
  "ds_read_b64 v[132:133], v34 offset:1024\n" \
  "ds_read_b64 v[134:135], v34 offset:1280\n" \
  "ds_read_b64 v[136:137], v34 offset:1536\n" \
  "s_waitcnt lgkmcnt(5)\n" \
  "v_add_f32 v124, v124, v126\n" \
  "v_add_f32 v125, v125, v127\n" \
  "s_waitcnt lgkmcnt(3)\n" \
  "v_add_f32 v128, v128, v130\n" \
  "v_add_f32 v129, v129, v131\n" \
  "s_waitcnt lgkmcnt(1)\n" \
  "v_add_f32 v132, v132, v134\n" \
  "v_add_f32 v133, v133, v135\n" \
  "v_add_f32 v124, v124, v128\n" \
  "v_add_f32 v125, v125, v129\n" \
  "s_waitcnt lgkmcnt(0)\n" \
  "v_add_f32 v132, v132, v136\n" \
  "v_add_f32 v133, v133, v137\n" \
  "v_add_f32 v124, v124, v132\n" \
  "v_add_f32 v125, v125, v133\n" \
  "s_mov_b32 exec_hi, 0\n" \
  "buffer_store_dwordx2 v[124:125], v32, s[28:31], s41 offen sc1\n" \
  "s_mov_b32 exec_hi, -1\n" \
  "SPJ:\n" \
  "s_nop 7\n" \
  "s_add_u32 s50, s50, 0x600000\n" \
  "s_add_u32 s51, s51, 0x600000\n" \
  "s_add_u32 s52, s52, 0x600000\n" \
  "s_add_u32 s53, s53, 0x600000\n" \
  "s_add_u32 s54, s54, 0x600000\n" \
  "s_mov_b32 s56, 1\n" \
  "s_sub_u32 s55, s55, 0x390000\n" \
  "s_mov_b32 s68, 0\n" \
  "v_add_u32 v2, 0x1c0000, v0\n" \
  "ds_read_b128 v[224:227], v0\n" \
  "ds_read_b128 v[228:231], v0 offset:1024\n" \
  "ds_read_b128 v[232:235], v0 offset:2048\n" \
  "ds_read_b128 v[236:239], v0 offset:3072\n" \
  "ds_read_b128 v[240:243], v0 offset:4096\n" \
  "ds_read_b128 v[244:247], v0 offset:5120\n" \
  "s_waitcnt vmcnt(4)\n" \
  "v_mfma_f32_16x16x32_bf16 v[60:63], v[168:171], v[16:19], 0\n" \
  "v_mfma_f32_16x16x32_bf16 v[68:71], v[168:171], v[20:23], 0\n" \
  "s_waitcnt vmcnt(3)\n" \
  "v_mfma_f32_16x16x32_bf16 v[76:79], v[172:175], v[16:19], 0\n" \
  "v_mfma_f32_16x16x32_bf16 v[84:87], v[172:175], v[20:23], 0\n" \
  "buffer_load_dwordx4 v[184:187], v1, s[28:31], s47 offen sc1\n" \
  "buffer_load_dwordx4 v[188:191], v1, s[28:31], s47 offen offset:1024 sc1\n" \
  "buffer_load_dwordx4 v[192:195], v1, s[28:31], s47 offen offset:2048 sc1\n" \
  "buffer_load_dwordx4 v[196:199], v1, s[28:31], s47 offen offset:3072 sc1\n" \
  "s_waitcnt vmcnt(6)\n" \
  "v_mfma_f32_16x16x32_bf16 v[92:95], v[176:179], v[16:19], 0\n" \
  "v_cvt_pk_bf16_f32 v168, v60, v61\n" \
  "v_cvt_pk_bf16_f32 v169, v62, v63\n" \
  "v_mfma_f32_16x16x32_bf16 v[100:103], v[176:179], v[20:23], 0\n" \
  "v_cvt_pk_bf16_f32 v170, v68, v69\n" \
  "v_cvt_pk_bf16_f32 v171, v70, v71\n" \
  "s_waitcnt vmcnt(5)\n" \
  "v_mfma_f32_16x16x32_bf16 v[60:63], v[180:183], v[16:19], 0\n" \
  "v_cvt_pk_bf16_f32 v172, v76, v77\n" \
  "v_cvt_pk_bf16_f32 v173, v78, v79\n" \
  "v_mfma_f32_16x16x32_bf16 v[68:71], v[180:183], v[20:23], 0\n" \
  "v_cvt_pk_bf16_f32 v174, v84, v85\n" \
  "v_cvt_pk_bf16_f32 v175, v86, v87\n" \
  "v_cvt_pk_bf16_f32 v176, v92, v93\n" \
  "v_cvt_pk_bf16_f32 v177, v94, v95\n" \
  "v_cvt_pk_bf16_f32 v178, v100, v101\n" \
  "v_cvt_pk_bf16_f32 v179, v102, v103\n" \
  "v_cvt_pk_bf16_f32 v180, v60, v61\n" \
  "v_cvt_pk_bf16_f32 v181, v62, v63\n" \
  "v_cvt_pk_bf16_f32 v182, v68, v69\n" \
  "v_cvt_pk_bf16_f32 v183, v70, v71\n" \
  "s_waitcnt vmcnt(4)\n" \
  "buffer_load_dwordx4 v[56:59], v2, s[28:31], s46 offen sc1\n" \
  "buffer_load_dwordx4 v[64:67], v2, s[28:31], s46 offen offset:1024 sc1\n" \
  "buffer_load_dwordx4 v[72:75], v2, s[28:31], s46 offen offset:2048 sc1\n" \
  "buffer_load_dwordx4 v[80:83], v2, s[28:31], s46 offen offset:3072 sc1\n" \
  "buffer_load_dwordx4 v[88:91], v2, s[28:31], s47 offen sc1\n" \
  "buffer_load_dwordx2 v[156:157], v48, s[28:31], s40 offen sc1\n" \
  "buffer_load_dwordx4 v[96:99], v2, s[28:31], s47 offen offset:1024 sc1\n" \
  "s_setprio 2\n" \
  "C_st2:\n" \
  "s_waitcnt vmcnt(1)\n" \
  "v_cmp_eq_u32 vcc, -1, v157\n" \
  "s_nop 0\n" \
  "s_cbranch_vccnz F_st2\n" \
  "K_st2:\n" \
  "v_permlane32_swap_b32 v156, v157\n" \
  "v_add_f32 v156, v156, v157\n" \
  "v_mov_b32 v157, v156\n" \
  "s_nop 1\n" \
  "v_permlane16_swap_b32 v156, v157\n" \
  "v_add_f32 v156, v156, v157\n" \
  "v_mov_b32 v157, v156\n" \
  "s_nop 1\n" \
  "v_permlane32_swap_b32 v156, v157\n" \
  "v_mul_f32 v156, 0x3b124925, v156\n" \
  "v_mul_f32 v157, 0x3b124925, v157\n" \
  "v_fma_f32 v157, -v156, v156, v157\n" \
  "v_max_f32 v157, 0, v157\n" \
  "v_cvt_pk_bf16_f32 v28, 0, v156\n" \
  "v_cvt_pk_bf16_f32 v157, 0, v157\n" \
  "v_add_f32 v30, 0x3727c5ac, v157\n" \
  "v_xor_b32 v252, 0x80000000, v28\n" \
  "v_cvt_pk_bf16_f32 v30, 0, v30\n" \
  "v_xor_b32 v253, 0x80000000, v28\n" \
  "v_sqrt_f32 v30, v30\n" \
  "v_xor_b32 v254, 0x80000000, v28\n" \
  "v_cvt_pk_bf16_f32 v30, 0, v30\n" \
  "v_xor_b32 v255, 0x80000000, v28\n" \
  "v_mov_b32 v248, v30\n" \
  "v_mov_b32 v249, v30\n" \
  "v_mov_b32 v250, v30\n" \
  "v_mov_b32 v251, v30\n" \
  "C_x2_0:\n" \
  "v_max3_u32 v34, v57, v59, v65\n" \
  "v_max_u32 v34, v34, v67\n" \
  "v_cmp_eq_u32 vcc, -1, v34\n" \
  "v_mfma_f32_4x4x4_16b_bf16 v[60:63], v[14:15], v[56:57], v[252:255]\n" \
  "v_mfma_f32_4x4x4_16b_bf16 v[68:71], v[14:15], v[58:59], v[252:255]\n" \
  "s_cbranch_vccnz F_x2_0\n" \
  "K_x2_0:\n" \
  "v_rcp_f32 v29, v30\n" \
  "v_mfma_f32_4x4x4_16b_bf16 v[76:79], v[14:15], v[168:169], v[248:251]\n" \
  "v_mfma_f32_4x4x4_16b_bf16 v[84:87], v[14:15], v[170:171], v[248:251]\n" \
  "v_mfma_f32_4x4x4_16b_bf16 v[92:95], v[14:15], v[64:65], v[252:255]\n" \
  "v_mfma_f32_4x4x4_16b_bf16 v[100:103], v[14:15], v[66:67], v[252:255]\n" \
  "v_mul_f32 v31, v29, v29\n" \
  "v_mfma_f32_4x4x4_16b_bf16 v[108:111], v[14:15], v[172:173], v[248:251]\n" \
  "v_mfma_f32_4x4x4_16b_bf16 v[116:119], v[14:15], v[174:175], v[248:251]\n" \
  "ds_write_b64 v8, v[28:29] offset:1024\n" \
  "v_mul_f32 v60, v60, v76\n" \
  "v_mul_f32 v61, v61, v77\n" \
  "v_mul_f32 v62, v62, v78\n" \
  "v_mul_f32 v63, v63, v79\n" \
  "v_cvt_pk_bf16_f32 v56, v60, v61\n" \
  "v_mul_f32 v68, v68, v84\n" \
  "v_mul_f32 v69, v69, v85\n" \
  "v_cvt_pk_bf16_f32 v57, v62, v63\n" \
  "v_mul_f32 v70, v70, v86\n" \
  "v_mul_f32 v71, v71, v87\n" \
  "v_cvt_pk_bf16_f32 v58, v68, v69\n" \
  "v_cvt_pk_bf16_f32 v59, v70, v71\n" \
  "v_max3_u32 v34, v73, v75, v81\n" \
  "v_max_u32 v34, v34, v83\n" \
  "v_cmp_eq_u32 vcc, -1, v34\n" \
  "v_mfma_f32_4x4x4_16b_bf16 v[76:79], v[14:15], v[176:177], v[248:251]\n" \
  "v_mul_f32 v92, v92, v108\n" \
  "v_mfma_f32_4x4x4_16b_bf16 v[84:87], v[14:15], v[178:179], v[248:251]\n" \
  "v_mul_f32 v93, v93, v109\n" \
  "s_cbranch_vccnz F_x2_2\n" \
  "K_x2_2:\n" \
  "v_mfma_f32_4x4x4_16b_bf16 v[60:63], v[14:15], v[72:73], v[252:255]\n" \
  "v_mul_f32 v94, v94, v110\n" \
  "v_mfma_f32_4x4x4_16b_bf16 v[68:71], v[14:15], v[74:75], v[252:255]\n" \
  "v_mul_f32 v95, v95, v111\n" \
  "v_mfma_f32_16x16x32_bf16 v[44:47], v[56:59], v[56:59], 0\n" \
  "ds_read_b128 v[168:171], v0 offset:28672\n" \
  "ds_read_b128 v[124:127], v0 offset:49152\n" \
  "buffer_load_dwordx4 v[104:107], v2, s[28:31], s47 offen offset:2048 sc1\n" \
  "v_cvt_pk_bf16_f32 v64, v92, v93\n" \
  "v_mul_f32 v100, v100, v116\n" \
  "s_waitcnt lgkmcnt(7)\n" \
  "v_mfma_f32_16x16x32_bf16 v[52:55], v[228:231], v[56:59], 0\n" \
  "ds_read_b128 v[228:231], v0 offset:7168\n" \
  "buffer_load_dwordx4 v[112:115], v2, s[28:31], s47 offen offset:3072 sc1\n" \
  "v_mul_f32 v101, v101, v117\n" \
  "v_cvt_pk_bf16_f32 v65, v94, v95\n" \
  "v_mfma_f32_16x16x32_bf16 v[48:51], v[224:227], v[56:59], 0\n" \
  "ds_read_b128 v[224:227], v0 offset:6144\n" \
  "v_mul_f32 v102, v102, v118\n" \
  "v_mul_f32 v103, v103, v119\n" \
  "v_cvt_pk_bf16_f32 v66, v100, v101\n" \
  "v_cvt_pk_bf16_f32 v67, v102, v103\n" \
  "v_mfma_f32_4x4x4_16b_bf16 v[108:111], v[14:15], v[180:181], v[248:251]\n" \
  "v_mul_f32 v60, v60, v76\n" \
  "v_mfma_f32_4x4x4_16b_bf16 v[116:119], v[14:15], v[182:183], v[248:251]\n" \
  "v_mul_f32 v61, v61, v77\n" \
  "v_mfma_f32_4x4x4_16b_bf16 v[92:95], v[14:15], v[80:81], v[252:255]\n" \
  "v_mul_f32 v62, v62, v78\n" \
  "v_mfma_f32_4x4x4_16b_bf16 v[100:103], v[14:15], v[82:83], v[252:255]\n" \
  "v_mul_f32 v63, v63, v79\n" \
  "v_mfma_f32_16x16x32_bf16 v[44:47], v[64:67], v[64:67], v[44:47]\n" \
  "ds_read_b128 v[172:175], v0 offset:29696\n" \
  "ds_read_b128 v[132:135], v0 offset:50176\n" \
  "buffer_load_dwordx4 v[120:123], v2, s[28:31], s48 offen sc1\n" \
  "v_cvt_pk_bf16_f32 v72, v60, v61\n" \
  "v_mul_f32 v68, v68, v84\n" \
  "s_waitcnt lgkmcnt(9)\n" \
  "v_mfma_f32_16x16x32_bf16 v[52:55], v[236:239], v[64:67], v[52:55]\n" \
  "ds_read_b128 v[236:239], v0 offset:9216\n" \
  "buffer_load_dwordx4 v[200:203], v1, s[28:31], s48 offen sc1\n" \
  "v_mul_f32 v69, v69, v85\n" \
  "v_cvt_pk_bf16_f32 v73, v62, v63\n" \
  "v_mfma_f32_16x16x32_bf16 v[48:51], v[232:235], v[64:67], v[48:51]\n" \
  "ds_read_b128 v[232:235], v0 offset:8192\n" \
  "v_mul_f32 v70, v70, v86\n" \
  "v_mul_f32 v71, v71, v87\n" \
  "v_cvt_pk_bf16_f32 v74, v68, v69\n" \
  "v_cvt_pk_bf16_f32 v75, v70, v71\n" \
  "v_mfma_f32_16x16x32_bf16 v[76:79], v[184:187], v[16:19], v[248:251]\n" \
  "v_mul_f32 v92, v92, v108\n" \
  "v_mul_f32 v93, v93, v109\n" \
  "v_mfma_f32_16x16x32_bf16 v[84:87], v[184:187], v[20:23], v[248:251]\n" \
  "s_waitcnt vmcnt(4)\n" \
  "v_max3_u32 v34, v89, v91, v97\n" \
  "v_max_u32 v34, v34, v99\n" \
  "v_cmp_eq_u32 vcc, -1, v34\n" \
  "v_mul_f32 v94, v94, v110\n" \
  "s_cbranch_vccnz F_x2_4\n" \
  "K_x2_4:\n" \
  "v_mfma_f32_4x4x4_16b_bf16 v[60:63], v[14:15], v[88:89], v[252:255]\n" \
  "v_mul_f32 v95, v95, v111\n" \
  "v_mfma_f32_4x4x4_16b_bf16 v[68:71], v[14:15], v[90:91], v[252:255]\n" \
  "v_cvt_pk_bf16_f32 v80, v92, v93\n" \
  "v_mfma_f32_16x16x32_bf16 v[44:47], v[72:75], v[72:75], v[44:47]\n" \
  "ds_read_b128 v[176:179], v0 offset:30720\n" \
  "buffer_load_dwordx4 v[128:131], v2, s[28:31], s48 offen offset:1024 sc1\n" \
  "v_mul_f32 v100, v100, v116\n" \
  "v_mul_f32 v101, v101, v117\n" \
  "s_waitcnt lgkmcnt(10)\n" \
  "v_mfma_f32_16x16x32_bf16 v[52:55], v[244:247], v[72:75], v[52:55]\n" \
  "ds_read_b128 v[244:247], v0 offset:11264\n" \
  "buffer_load_dwordx4 v[204:207], v1, s[28:31], s48 offen offset:1024 sc1\n" \
  "v_cvt_pk_bf16_f32 v81, v94, v95\n" \
  "v_mul_f32 v102, v102, v118\n" \
  "v_mfma_f32_16x16x32_bf16 v[48:51], v[240:243], v[72:75], v[48:51]\n" \
  "ds_read_b128 v[240:243], v0 offset:10240\n" \
  "v_mul_f32 v103, v103, v119\n" \
  "v_cvt_pk_bf16_f32 v82, v100, v101\n" \
  "v_cvt_pk_bf16_f32 v83, v102, v103\n" \
  "v_mfma_f32_16x16x32_bf16 v[108:111], v[188:191], v[16:19], v[248:251]\n" \
  "v_mul_f32 v60, v60, v76\n" \
  "v_mul_f32 v61, v61, v77\n" \
  "v_mfma_f32_16x16x32_bf16 v[116:119], v[188:191], v[20:23], v[248:251]\n" \
  "v_mul_f32 v62, v62, v78\n" \
  "v_mul_f32 v63, v63, v79\n" \
  "v_cvt_pk_bf16_f32 v88, v60, v61\n" \
  "v_mul_f32 v68, v68, v84\n" \
  "v_mfma_f32_4x4x4_16b_bf16 v[92:95], v[14:15], v[96:97], v[252:255]\n" \
  "v_mul_f32 v69, v69, v85\n" \
  "v_mfma_f32_4x4x4_16b_bf16 v[100:103], v[14:15], v[98:99], v[252:255]\n" \
  "v_cvt_pk_bf16_f32 v89, v62, v63\n" \
  "v_mfma_f32_16x16x32_bf16 v[44:47], v[80:83], v[80:83], v[44:47]\n" \
  "ds_read_b128 v[180:183], v0 offset:31744\n" \
  "buffer_load_dwordx4 v[136:139], v2, s[28:31], s48 offen offset:2048 sc1\n" \
  "v_mul_f32 v70, v70, v86\n" \
  "s_waitcnt lgkmcnt(9)\n" \
  "v_mfma_f32_16x16x32_bf16 v[52:55], v[228:231], v[80:83], v[52:55]\n" \
  "ds_read_b128 v[228:231], v0 offset:13312\n" \
  "buffer_load_dwordx4 v[208:211], v1, s[28:31], s48 offen offset:2048 sc1\n" \
  "v_mul_f32 v71, v71, v87\n" \
  "s_waitcnt lgkmcnt(9)\n" \
  "v_mfma_f32_16x16x32_bf16 v[48:51], v[224:227], v[80:83], v[48:51]\n" \
  "ds_read_b128 v[224:227], v0 offset:12288\n" \
  "v_cvt_pk_bf16_f32 v90, v68, v69\n" \
  "v_cvt_pk_bf16_f32 v91, v70, v71\n" \
  "v_mfma_f32_16x16x32_bf16 v[76:79], v[192:195], v[16:19], v[248:251]\n" \
  "v_mul_f32 v92, v92, v108\n" \
  "v_mul_f32 v93, v93, v109\n" \
  "v_mfma_f32_16x16x32_bf16 v[84:87], v[192:195], v[20:23], v[248:251]\n" \
  "s_waitcnt vmcnt(6)\n" \
  "v_max3_u32 v34, v105, v107, v113\n" \
  "v_max_u32 v34, v34, v115\n" \
  "v_cmp_eq_u32 vcc, -1, v34\n" \
  "v_mul_f32 v94, v94, v110\n" \
  "s_cbranch_vccnz F_x2_6\n" \
  "K_x2_6:\n" \
  "v_mfma_f32_4x4x4_16b_bf16 v[60:63], v[14:15], v[104:105], v[252:255]\n" \
  "v_mul_f32 v95, v95, v111\n" \
  "v_mfma_f32_4x4x4_16b_bf16 v[68:71], v[14:15], v[106:107], v[252:255]\n" \
  "v_cvt_pk_bf16_f32 v96, v92, v93\n" \
  "v_mfma_f32_16x16x32_bf16 v[44:47], v[88:91], v[88:91], v[44:47]\n" \
  "ds_read_b128 v[184:187], v0 offset:32768\n" \
  "buffer_load_dwordx4 v[144:147], v2, s[28:31], s48 offen offset:3072 sc1\n" \
  "v_mul_f32 v100, v100, v116\n" \
  "v_mul_f32 v101, v101, v117\n" \
  "s_waitcnt lgkmcnt(8)\n" \
  "v_mfma_f32_16x16x32_bf16 v[52:55], v[236:239], v[88:91], v[52:55]\n" \
  "ds_read_b128 v[236:239], v0 offset:15360\n" \
  "buffer_load_dwordx4 v[212:215], v1, s[28:31], s48 offen offset:3072 sc1\n" \
  "v_cvt_pk_bf16_f32 v97, v94, v95\n" \
  "v_mul_f32 v102, v102, v118\n" \
  "s_waitcnt lgkmcnt(8)\n" \
  "v_mfma_f32_16x16x32_bf16 v[48:51], v[232:235], v[88:91], v[48:51]\n" \
  "ds_read_b128 v[232:235], v0 offset:14336\n" \
  "v_mul_f32 v103, v103, v119\n" \
  "v_cvt_pk_bf16_f32 v98, v100, v101\n" \
  "v_cvt_pk_bf16_f32 v99, v102, v103\n" \
  "v_mfma_f32_16x16x32_bf16 v[108:111], v[196:199], v[16:19], v[248:251]\n" \
  "v_mul_f32 v60, v60, v76\n" \
  "v_mul_f32 v61, v61, v77\n" \
  "v_mfma_f32_16x16x32_bf16 v[116:119], v[196:199], v[20:23], v[248:251]\n" \
  "v_mul_f32 v62, v62, v78\n" \
  "v_mul_f32 v63, v63, v79\n" \
  "v_cvt_pk_bf16_f32 v104, v60, v61\n" \
  "v_mul_f32 v68, v68, v84\n" \
  "v_mfma_f32_4x4x4_16b_bf16 v[92:95], v[14:15], v[112:113], v[252:255]\n" \
  "v_mul_f32 v69, v69, v85\n" \
  "v_mfma_f32_4x4x4_16b_bf16 v[100:103], v[14:15], v[114:115], v[252:255]\n" \
  "v_cvt_pk_bf16_f32 v105, v62, v63\n" \
  "v_mfma_f32_16x16x32_bf16 v[44:47], v[96:99], v[96:99], v[44:47]\n" \
  "ds_read_b128 v[188:191], v0 offset:33792\n" \
  "buffer_load_dwordx4 v[152:155], v2, s[28:31], s49 offen sc1\n" \
  "v_mul_f32 v70, v70, v86\n" \
  "s_waitcnt lgkmcnt(8)\n" \
  "v_mfma_f32_16x16x32_bf16 v[52:55], v[244:247], v[96:99], v[52:55]\n" \
  "ds_read_b128 v[244:247], v0 offset:17408\n" \
  "buffer_load_dwordx4 v[216:219], v1, s[28:31], s49 offen sc1\n" \
  "v_mul_f32 v71, v71, v87\n" \
  "s_waitcnt lgkmcnt(8)\n" \
  "v_mfma_f32_16x16x32_bf16 v[48:51], v[240:243], v[96:99], v[48:51]\n" \
  "ds_read_b128 v[240:243], v0 offset:16384\n" \
  "v_cvt_pk_bf16_f32 v106, v68, v69\n" \
  "v_cvt_pk_bf16_f32 v107, v70, v71\n" \
  "s_waitcnt vmcnt(8)\n" \
  "v_mfma_f32_16x16x32_bf16 v[76:79], v[200:203], v[16:19], v[248:251]\n" \
  "v_mul_f32 v92, v92, v108\n" \
  "v_mul_f32 v93, v93, v109\n" \
  "v_mfma_f32_16x16x32_bf16 v[84:87], v[200:203], v[20:23], v[248:251]\n" \
  "s_waitcnt vmcnt(7)\n" \
  "v_max3_u32 v34, v121, v123, v129\n" \
  "v_max_u32 v34, v34, v131\n" \
  "v_cmp_eq_u32 vcc, -1, v34\n" \
  "v_mul_f32 v94, v94, v110\n" \
  "s_cbranch_vccnz F_x2_8\n" \
  "K_x2_8:\n" \
  "v_mfma_f32_4x4x4_16b_bf16 v[60:63], v[14:15], v[120:121], v[252:255]\n" \
  "v_mul_f32 v95, v95, v111\n" \
  "v_mfma_f32_4x4x4_16b_bf16 v[68:71], v[14:15], v[122:123], v[252:255]\n" \
  "v_cvt_pk_bf16_f32 v112, v92, v93\n" \
  "v_mfma_f32_16x16x32_bf16 v[44:47], v[104:107], v[104:107], v[44:47]\n" \
  "ds_read_b128 v[192:195], v0 offset:34816\n" \
  "buffer_load_dwordx4 v[160:163], v2, s[28:31], s49 offen offset:1024 sc1\n" \
  "v_mul_f32 v100, v100, v116\n" \
  "v_mul_f32 v101, v101, v117\n" \
  "s_waitcnt lgkmcnt(8)\n" \
  "v_mfma_f32_16x16x32_bf16 v[52:55], v[228:231], v[104:107], v[52:55]\n" \
  "ds_read_b128 v[228:231], v0 offset:19456\n" \
  "buffer_load_dwordx4 v[220:223], v1, s[28:31], s49 offen offset:1024 sc1\n" \
  "v_cvt_pk_bf16_f32 v113, v94, v95\n" \
  "v_mul_f32 v102, v102, v118\n" \
  "s_waitcnt lgkmcnt(8)\n" \
  "v_mfma_f32_16x16x32_bf16 v[48:51], v[224:227], v[104:107], v[48:51]\n" \
  "ds_read_b128 v[224:227], v0 offset:18432\n" \
  "v_mul_f32 v103, v103, v119\n" \
  "v_cvt_pk_bf16_f32 v114, v100, v101\n" \
  "v_cvt_pk_bf16_f32 v115, v102, v103\n" \
  "s_waitcnt vmcnt(8)\n" \
  "v_mfma_f32_16x16x32_bf16 v[108:111], v[204:207], v[16:19], v[248:251]\n" \
  "v_mul_f32 v60, v60, v76\n" \
  "v_mul_f32 v61, v61, v77\n" \
  "v_mfma_f32_16x16x32_bf16 v[116:119], v[204:207], v[20:23], v[248:251]\n" \
  "v_mul_f32 v62, v62, v78\n" \
  "v_mul_f32 v63, v63, v79\n" \
  "v_cvt_pk_bf16_f32 v120, v60, v61\n" \
  "v_mul_f32 v68, v68, v84\n" \
  "v_mfma_f32_4x4x4_16b_bf16 v[92:95], v[14:15], v[128:129], v[252:255]\n" \
  "v_mul_f32 v69, v69, v85\n" \
  "v_mfma_f32_4x4x4_16b_bf16 v[100:103], v[14:15], v[130:131], v[252:255]\n" \
  "v_cvt_pk_bf16_f32 v121, v62, v63\n" \
  "v_mfma_f32_16x16x32_bf16 v[44:47], v[112:115], v[112:115], v[44:47]\n" \
  "ds_read_b128 v[196:199], v0 offset:35840\n" \
  "v_mul_f32 v70, v70, v86\n" \
  "s_waitcnt lgkmcnt(8)\n" \
  "v_mfma_f32_16x16x32_bf16 v[52:55], v[236:239], v[112:115], v[52:55]\n" \
  "ds_read_b128 v[236:239], v0 offset:21504\n" \
  "v_mul_f32 v71, v71, v87\n" \
  "s_waitcnt lgkmcnt(8)\n" \
  "v_mfma_f32_16x16x32_bf16 v[48:51], v[232:235], v[112:115], v[48:51]\n" \
  "ds_read_b128 v[232:235], v0 offset:20480\n" \
  "v_cvt_pk_bf16_f32 v122, v68, v69\n" \
  "v_cvt_pk_bf16_f32 v123, v70, v71\n" \
  "s_waitcnt vmcnt(6)\n" \
  "v_mfma_f32_16x16x32_bf16 v[76:79], v[208:211], v[16:19], v[248:251]\n" \
  "v_mul_f32 v92, v92, v108\n" \
  "v_mul_f32 v93, v93, v109\n" \
  "v_mfma_f32_16x16x32_bf16 v[84:87], v[208:211], v[20:23], v[248:251]\n" \
  "s_waitcnt vmcnt(5)\n" \
  "v_max3_u32 v34, v137, v139, v145\n" \
  "v_max_u32 v34, v34, v147\n" \
  "v_cmp_eq_u32 vcc, -1, v34\n" \
  "v_mul_f32 v94, v94, v110\n" \
  "s_cbranch_vccnz F_x2_10\n" \
  "K_x2_10:\n" \
  "v_mfma_f32_4x4x4_16b_bf16 v[60:63], v[14:15], v[136:137], v[252:255]\n" \
  "v_mul_f32 v95, v95, v111\n" \
  "v_mfma_f32_4x4x4_16b_bf16 v[68:71], v[14:15], v[138:139], v[252:255]\n" \
  "v_cvt_pk_bf16_f32 v128, v92, v93\n" \
  "v_mfma_f32_16x16x32_bf16 v[44:47], v[120:123], v[120:123], v[44:47]\n" \
  "ds_read_b128 v[200:203], v0 offset:36864\n" \
  "v_mul_f32 v100, v100, v116\n" \
  "v_mul_f32 v101, v101, v117\n" \
  "s_waitcnt lgkmcnt(8)\n" \
  "v_mfma_f32_16x16x32_bf16 v[52:55], v[244:247], v[120:123], v[52:55]\n" \
  "ds_read_b128 v[244:247], v0 offset:23552\n" \
  "v_cvt_pk_bf16_f32 v129, v94, v95\n" \
  "v_mul_f32 v102, v102, v118\n" \
  "s_waitcnt lgkmcnt(8)\n" \
  "v_mfma_f32_16x16x32_bf16 v[48:51], v[240:243], v[120:123], v[48:51]\n" \
  "ds_read_b128 v[240:243], v0 offset:22528\n" \
  "v_mul_f32 v103, v103, v119\n" \
  "v_cvt_pk_bf16_f32 v130, v100, v101\n" \
  "v_cvt_pk_bf16_f32 v131, v102, v103\n" \
  "s_waitcnt vmcnt(4)\n" \
  "v_mfma_f32_16x16x32_bf16 v[108:111], v[212:215], v[16:19], v[248:251]\n" \
  "v_mul_f32 v60, v60, v76\n" \
  "v_mul_f32 v61, v61, v77\n" \
  "v_mfma_f32_16x16x32_bf16 v[116:119], v[212:215], v[20:23], v[248:251]\n" \
  "v_mul_f32 v62, v62, v78\n" \
  "v_mul_f32 v63, v63, v79\n" \
  "v_cvt_pk_bf16_f32 v136, v60, v61\n" \
  "v_mul_f32 v68, v68, v84\n" \
  "v_mfma_f32_4x4x4_16b_bf16 v[92:95], v[14:15], v[144:145], v[252:255]\n" \
  "v_mul_f32 v69, v69, v85\n" \
  "v_mfma_f32_4x4x4_16b_bf16 v[100:103], v[14:15], v[146:147], v[252:255]\n" \
  "v_cvt_pk_bf16_f32 v137, v62, v63\n" \
  "v_mfma_f32_16x16x32_bf16 v[44:47], v[128:131], v[128:131], v[44:47]\n" \
  "ds_read_b128 v[204:207], v0 offset:37888\n" \
  "v_mul_f32 v70, v70, v86\n" \
  "s_waitcnt lgkmcnt(8)\n" \
  "v_mfma_f32_16x16x32_bf16 v[52:55], v[228:231], v[128:131], v[52:55]\n" \
  "ds_read_b128 v[228:231], v0 offset:25600\n" \
  "v_mul_f32 v71, v71, v87\n" \
  "s_waitcnt lgkmcnt(8)\n" \
  "v_mfma_f32_16x16x32_bf16 v[48:51], v[224:227], v[128:131], v[48:51]\n" \
  "ds_read_b128 v[224:227], v0 offset:24576\n" \
  "v_cvt_pk_bf16_f32 v138, v68, v69\n" \
  "v_cvt_pk_bf16_f32 v139, v70, v71\n" \
  "s_waitcnt vmcnt(2)\n" \
  "v_mfma_f32_16x16x32_bf16 v[76:79], v[216:219], v[16:19], v[248:251]\n" \
  "v_mul_f32 v92, v92, v108\n" \
  "v_mul_f32 v93, v93, v109\n" \
  "v_mfma_f32_16x16x32_bf16 v[84:87], v[216:219], v[20:23], v[248:251]\n" \
  "s_waitcnt vmcnt(1)\n" \
  "v_max3_u32 v34, v153, v155, v161\n" \
  "v_max_u32 v34, v34, v163\n" \
  "v_cmp_eq_u32 vcc, -1, v34\n" \
  "v_mul_f32 v94, v94, v110\n" \
  "s_cbranch_vccnz F_x2_12\n" \
  "K_x2_12:\n" \
  "v_mfma_f32_4x4x4_16b_bf16 v[60:63], v[14:15], v[152:153], v[252:255]\n" \
  "v_mul_f32 v95, v95, v111\n" \
  "v_mfma_f32_4x4x4_16b_bf16 v[68:71], v[14:15], v[154:155], v[252:255]\n" \
  "v_cvt_pk_bf16_f32 v144, v92, v93\n" \
  "v_mfma_f32_16x16x32_bf16 v[44:47], v[136:139], v[136:139], v[44:47]\n" \
  "ds_read_b128 v[208:211], v0 offset:38912\n" \
  "v_mul_f32 v100, v100, v116\n" \
  "v_mul_f32 v101, v101, v117\n" \
  "s_waitcnt lgkmcnt(8)\n" \
  "v_mfma_f32_16x16x32_bf16 v[52:55], v[236:239], v[136:139], v[52:55]\n" \
  "ds_read_b128 v[236:239], v0 offset:27648\n" \
  "v_cvt_pk_bf16_f32 v145, v94, v95\n" \
  "v_mul_f32 v102, v102, v118\n" \
  "s_waitcnt lgkmcnt(8)\n" \
  "v_mfma_f32_16x16x32_bf16 v[48:51], v[232:235], v[136:139], v[48:51]\n" \
  "ds_read_b128 v[232:235], v0 offset:26624\n" \
  "v_mul_f32 v103, v103, v119\n" \
  "v_cvt_pk_bf16_f32 v146, v100, v101\n" \
  "v_cvt_pk_bf16_f32 v147, v102, v103\n" \
  "s_waitcnt vmcnt(0)\n" \
  "v_mfma_f32_16x16x32_bf16 v[108:111], v[220:223], v[16:19], v[248:251]\n" \
  "v_mul_f32 v60, v60, v76\n" \
  "v_mul_f32 v61, v61, v77\n" \
  "v_mfma_f32_16x16x32_bf16 v[116:119], v[220:223], v[20:23], v[248:251]\n" \
  "v_mul_f32 v62, v62, v78\n" \
  "v_mul_f32 v63, v63, v79\n" \
  "v_cvt_pk_bf16_f32 v152, v60, v61\n" \
  "v_mul_f32 v68, v68, v84\n" \
  "v_mfma_f32_4x4x4_16b_bf16 v[92:95], v[14:15], v[160:161], v[252:255]\n" \
  "v_mul_f32 v69, v69, v85\n" \
  "v_mfma_f32_4x4x4_16b_bf16 v[100:103], v[14:15], v[162:163], v[252:255]\n" \
  "v_cvt_pk_bf16_f32 v153, v62, v63\n" \
  "v_mfma_f32_16x16x32_bf16 v[44:47], v[144:147], v[144:147], v[44:47]\n" \
  "ds_read_b128 v[212:215], v0 offset:39936\n" \
  "v_mul_f32 v70, v70, v86\n" \
  "s_waitcnt lgkmcnt(8)\n" \
  "v_mfma_f32_16x16x32_bf16 v[52:55], v[244:247], v[144:147], v[52:55]\n" \
  "ds_read_b128 v[244:247], v0 offset:44032\n" \
  "v_mul_f32 v71, v71, v87\n" \
  "s_waitcnt lgkmcnt(8)\n" \
  "v_mfma_f32_16x16x32_bf16 v[48:51], v[240:243], v[144:147], v[48:51]\n" \
  "ds_read_b128 v[240:243], v0 offset:43008\n" \
  "v_cvt_pk_bf16_f32 v154, v68, v69\n" \
  "v_cvt_pk_bf16_f32 v155, v70, v71\n" \
  "s_nop 1\n" \
  "v_mfma_f32_16x16x32_bf16 v[44:47], v[152:155], v[152:155], v[44:47]\n" \
  "ds_read_b128 v[216:219], v0 offset:40960\n" \
  "v_mul_f32 v92, v92, v108\n" \
  "v_mul_f32 v93, v93, v109\n" \
  "v_mul_f32 v94, v94, v110\n" \
  "v_mul_f32 v95, v95, v111\n" \
  "s_waitcnt lgkmcnt(8)\n" \
  "v_mfma_f32_16x16x32_bf16 v[52:55], v[228:231], v[152:155], v[52:55]\n" \
  "ds_read_b128 v[228:231], v0 offset:46080\n" \
  "v_cvt_pk_bf16_f32 v160, v92, v93\n" \
  "v_mul_f32 v100, v100, v116\n" \
  "v_mul_f32 v101, v101, v117\n" \
  "v_cvt_pk_bf16_f32 v161, v94, v95\n" \
  "s_waitcnt lgkmcnt(8)\n" \
  "v_mfma_f32_16x16x32_bf16 v[48:51], v[224:227], v[152:155], v[48:51]\n" \
  "ds_read_b128 v[224:227], v0 offset:45056\n" \
  "v_mul_f32 v102, v102, v118\n" \
  "v_mul_f32 v103, v103, v119\n" \
  "v_cvt_pk_bf16_f32 v162, v100, v101\n" \
  "v_cvt_pk_bf16_f32 v163, v102, v103\n" \
  "s_nop 1\n" \
  "v_mfma_f32_16x16x32_bf16 v[44:47], v[160:163], v[160:163], v[44:47]\n" \
  "ds_read_b128 v[220:223], v0 offset:41984\n" \
  "s_waitcnt lgkmcnt(8)\n" \
  "v_mfma_f32_16x16x32_bf16 v[52:55], v[236:239], v[160:163], v[52:55]\n" \
  "ds_read_b128 v[236:239], v0 offset:48128\n" \
  "s_waitcnt lgkmcnt(8)\n" \
  "v_mfma_f32_16x16x32_bf16 v[48:51], v[232:235], v[160:163], v[48:51]\n" \
  "ds_read_b128 v[232:235], v0 offset:47104\n" \
  "s_branch COMMON\n" \
  "FIN:\n" \
  "buffer_store_dwordx4 v[124:127], v47, s[32:35], s42 offen\n" \
  "FIN7:\n" \
  "s_endpgm\n" \
  "SPARE:\n" \
  "s_mov_b64 s[28:29], s[24:25]\n" \
  "buffer_load_dword v180, off, s[28:31], 0 sc1\n" \
  "s_lshl_b32 s58, s65, 3\n" \
  "s_add_u32 s58, s58, %[w]\n" \
  "s_mul_i32 s58, s58, 0x1c00\n" \
  "v_lshrrev_b32 v32, 3, v176\n" \
  "v_mul_u32_u24 v32, 0x380, v32\n" \
  "v_and_b32 v34, 0x70, v0\n" \
  "v_add3_u32 v32, v32, v34, s58\n" \
  "global_load_dwordx4 v[56:59], v32, s[8:9]\n" \
  "global_load_dwordx4 v[64:67], v32, s[8:9] offset:128\n" \
  "global_load_dwordx4 v[72:75], v32, s[8:9] offset:256\n" \
  "global_load_dwordx4 v[80:83], v32, s[8:9] offset:384\n" \
  "global_load_dwordx4 v[88:91], v32, s[8:9] offset:512\n" \
  "global_load_dwordx4 v[96:99], v32, s[8:9] offset:640\n" \
  "global_load_dwordx4 v[104:107], v32, s[8:9] offset:768\n" \
  "s_lshl_b32 s66, s64, 5\n" \
  "s_add_u32 s66, s66, s65\n" \
  "s_lshl_b32 s58, s66, 4\n" \
  "v_lshlrev_b32 v32, 8, v0\n" \
  "s_cmp_eq_u32 %[w], 1\n" \
  "s_cselect_b64 exec, 14, 0\n" \
  "buffer_load_dword v213, v32, s[28:31], s58 offen sc1\n" \
  "s_mov_b64 exec, -1\n" \
  "s_lshr_b32 s58, s65, 2\n" \
  "s_lshl_b32 s58, s58, 11\n" \
  "s_and_b32 s59, s65, 3\n" \
  "s_lshl_b32 s59, s59, 8\n" \
  "s_add_u32 s58, s58, s59\n" \
  "s_add_u32 s58, s58, 0x950000\n" \
  "v_lshlrev_b32 v32, 3, v0\n" \
  "s_cmp_eq_u32 %[w], 2\n" \
  "s_cselect_b64 exec, 0x303, 0\n" \
  "buffer_load_dword v214, v32, s[28:31], s58 offen sc1\n" \
  "s_mov_b64 exec, -1\n" \
  "s_lshl_b32 s71, s66, 13\n" \
  "s_add_u32 s71, s71, 0x790000\n" \
  "v_mov_b32 v48, -1\n" \
  "v_mov_b32 v49, -1\n" \
  "v_mov_b32 v50, -1\n" \
  "v_mov_b32 v51, -1\n" \
  "s_lshr_b32 s36, s64, 1\n" \
  "s_lshl_b32 s36, s36, 2\n" \
  "s_lshr_b32 s33, s65, 3\n" \
  "s_add_u32 s36, s36, s33\n" \
  "s_lshl_b32 s33, s36, 3\n" \
  "s_add_u32 s33, s33, %[w]\n" \
  "s_mul_i32 s37, s33, 0x3800\n" \
  "s_and_b32 s46, s64, 1\n" \
  "s_lshl_b32 s46, s46, 3\n" \
  "s_and_b32 s58, s65, 7\n" \
  "s_add_u32 s46, s46, s58\n" \
  "s_lshr_b32 s47, s46, 2\n" \
  "s_and_b32 s48, s46, 3\n" \
  "s_min_u32 s49, %[w], 6\n" \
  "s_mul_i32 s58, s48, 7\n" \
  "s_add_u32 s49, s49, s58\n" \
  "s_lshl_b32 s58, s36, 3\n" \
  "s_add_u32 s58, s58, %[w]\n" \
  "s_lshl_b32 s40, s58, 9\n" \
  "s_add_u32 s40, s40, 0x780000\n" \
  "s_lshl_b32 s53, s58, 4\n" \
  "s_add_u32 s53, s53, s46\n" \
  "s_lshl_b32 s53, s53, 10\n" \
  "s_add_u32 s53, s53, 0xd90000\n" \
  "s_lshl_b32 s50, s66, 4\n" \
  "s_add_u32 s50, s50, %[w]\n" \
  "s_lshl_b32 s50, s50, 10\n" \
  "s_add_u32 s50, s50, 0x990000\n" \
  "s_mov_b32 s58, 0xa0000\n" \
  "s_cmp_lt_u32 s65, 8\n" \
  "s_cselect_b32 s58, s58, 0x20000\n" \
  "s_cmp_gt_u32 s36, 0\n" \
  "s_cselect_b32 s58, s58, 0\n" \
  "s_sub_u32 s51, s50, s58\n" \
  "s_mov_b32 s58, 0xc0000\n" \
  "s_cmp_lt_u32 s65, 16\n" \
  "s_cselect_b32 s58, s58, 0x40000\n" \
  "s_cmp_gt_u32 s36, 1\n" \
  "s_cselect_b32 s58, s58, 0\n" \
  "s_sub_u32 s52, s50, s58\n" \
  "s_lshl_b32 s58, s36, 2\n" \
  "s_add_u32 s58, s58, s47\n" \
  "s_lshl_b32 s54, s58, 3\n" \
  "s_add_u32 s54, s54, %[w]\n" \
  "s_lshl_b32 s54, s54, 12\n" \
  "s_add_u32 s54, s54, 0xd90000\n" \
  "s_mul_i32 s42, s58, 0x7000\n" \
  "s_lshl_b32 s38, s49, 9\n" \
  "s_add_u32 s38, s38, s42\n" \
  "s_lshl_b32 s41, s58, 10\n" \
  "s_lshl_b32 s48, s48, 7\n" \
  "s_add_u32 s41, s41, s48\n" \
  "s_add_u32 s41, s41, 0x780000\n" \
  "s_lshl_b32 s58, s49, 5\n" \
  "s_add_u32 s42, s42, s58\n" \
  "s_lshl_b32 s39, s49, 14\n" \
  "s_add_u32 s39, s39, 0x390000\n" \
  "s_lshl_b32 s43, s47, 8\n" \
  "s_add_u32 s55, s38, 0x790000\n" \
  "s_add_u32 s38, s38, 0x5c0000\n" \
  "s_mov_b32 s56, 0\n" \
  "v_add_u32 v26, 0x2000, v0\n" \
  "s_add_u32 s46, s37, 0x790000\n" \
  "s_add_u32 s47, s37, 0x791000\n" \
  "s_add_u32 s48, s37, 0x792000\n" \
  "s_add_u32 s49, s37, 0x793000\n" \
  "v_lshrrev_b32 v178, 4, v176\n" \
  "v_mov_b32 v212, 0x3f803f80\n" \
  "v_mov_b32 v196, 0\n" \
  "v_mov_b32 v197, 0\n" \
  "v_mov_b32 v198, 0\n" \
  "v_mov_b32 v199, 0\n" \
  "s_movk_i32 s57, 0x1000\n" \
  "s_lshr_b32 s12, %[w], 1\n" \
  "s_mul_i32 s12, s12, 0x7000\n" \
  "s_and_b32 s19, %[w], 1\n" \
  "s_lshl_b32 s19, s19, 10\n" \
  "s_add_u32 s12, s12, s19\n" \
  "s_and_b32 s19, s64, 1\n" \
  "s_lshl_b32 s19, s19, 3\n" \
  "s_and_b32 s58, s65, 7\n" \
  "s_add_u32 s19, s19, s58\n" \
  "s_mul_i32 s19, s19, 0x15000\n" \
  "s_add_u32 s19, s19, s12\n" \
  "s_add_u32 s19, s19, 0x1d0000\n" \
  "s_cmp_lt_u32 %[w], 6\n" \
  "s_cselect_b64 s[68:69], -1, 0\n" \
  "v_add_u32 v7, 0xe000, v0\n" \
  "s_lshl_b32 s58, %[w], 10\n" \
  "s_add_u32 s58, s58, 0x1b000\n" \
  "v_add_u32 v3, s58, v0\n" \
  "s_lshr_b32 s58, %[w], 1\n" \
  "s_lshl_b32 s58, s58, 11\n" \
  "s_add_u32 s58, s58, 0x15000\n" \
  "v_add_u32 v4, s58, v0\n" \
  "s_and_b32 s58, %[w], 1\n" \
  "s_lshl_b32 s58, s58, 3\n" \
  "v_add_u32 v5, s58, v4\n" \
  "s_lshl_b32 s58, %[w], 7\n" \
  "s_add_u32 s58, s58, 0x23000\n" \
  "v_lshl_add_u32 v8, v177, 3, s58\n" \
  "v_lshl_add_u32 v9, v178, 5, s58\n" \
  "s_mov_b32 s58, 0x27480\n" \
  "v_lshl_add_u32 v10, v178, 4, s58\n" \
  "v_lshl_add_u32 v11, v177, 2, s58\n" \
  "s_add_u32 s58, s43, 0x23400\n" \
  "v_lshl_add_u32 v24, v177, 3, s58\n" \
  "v_lshrrev_b32 v34, 1, v178\n" \
  "v_and_b32 v35, 1, v178\n" \
  "v_mul_u32_u24 v13, 0x3800, v35\n" \
  "v_lshl_add_u32 v13, v34, 8, v13\n" \
  "v_lshl_add_u32 v13, v177, 4, v13\n" \
  "v_lshlrev_b32 v27, 5, v177\n" \
  "v_lshl_add_u32 v27, v35, 4, v27\n" \
  "v_mul_u32_u24 v12, 0x3800, v34\n" \
  "v_add_u32 v12, v12, v27\n" \
  "v_lshl_add_u32 v1, v34, 7, v27\n" \
  "v_and_b32 v27, 0x180, v27\n" \
  "v_add_u32 v1, v1, v27\n" \
  "v_cmp_eq_u32 s[60:61], 3, v178\n" \
  "v_cmp_eq_u32 s[62:63], 12, v177\n" \
  "v_and_b32 v34, 1, v177\n" \
  "v_lshlrev_b32 v34, 4, v34\n" \
  "v_mov_b32 v35, 0x3f80\n" \
  "v_lshlrev_b32 v35, v34, v35\n" \
  "v_and_b32 v34, 2, v177\n" \
  "v_cmp_eq_u32 vcc, 0, v34\n" \
  "s_nop 1\n" \
  "v_cndmask_b32 v14, 0, v35, vcc\n" \
  "v_cndmask_b32 v15, v35, 0, vcc\n" \
  "v_lshlrev_b32 v27, 2, v178\n" \
  "v_lshrrev_b32 v34, 1, v177\n" \
  "v_sub_u32 v34, v34, v27\n" \
  "v_sub_u32 v27, v177, v27\n" \
  "v_cmp_eq_u32 vcc, 0, v34\n" \
  "s_nop 1\n" \
  "v_cndmask_b32 v16, 0, v35, vcc\n" \
  "v_cmp_eq_u32 vcc, -8, v34\n" \
  "s_nop 1\n" \
  "v_cndmask_b32 v20, 0, v35, vcc\n" \
  "v_cmp_eq_u32 vcc, 1, v34\n" \
  "s_nop 1\n" \
  "v_cndmask_b32 v17, 0, v35, vcc\n" \
  "v_cmp_eq_u32 vcc, -7, v34\n" \
  "s_nop 1\n" \
  "v_cndmask_b32 v21, 0, v35, vcc\n" \
  "v_cmp_eq_u32 vcc, 2, v34\n" \
  "s_nop 1\n" \
  "v_cndmask_b32 v18, 0, v35, vcc\n" \
  "v_cmp_eq_u32 vcc, -6, v34\n" \
  "s_nop 1\n" \
  "v_cndmask_b32 v22, 0, v35, vcc\n" \
  "v_cmp_eq_u32 vcc, 3, v34\n" \
  "s_nop 1\n" \
  "v_cndmask_b32 v19, 0, v35, vcc\n" \
  "v_cmp_eq_u32 vcc, -5, v34\n" \
  "s_nop 1\n" \
  "v_cndmask_b32 v23, 0, v35, vcc\n" \
  "v_cmp_eq_u32 s[44:45], 0, v27\n" \
  "s_and_b32 s33, s65, 1\n" \
  "s_mul_i32 s33, s33, 14\n" \
  "s_add_u32 s33, s33, %[w]\n" \
  "s_lshr_b32 s32, s65, 1\n" \
  "s_lshl_b32 s15, s33, 4\n" \
  "s_add_u32 s15, s15, s32\n" \
  "s_lshl_b32 s15, s15, 10\n" \
  "s_add_u32 s15, s15, 0x390000\n" \
  "s_lshl_b32 s33, s33, 5\n" \
  "s_add_u32 s33, s33, 0x15000\n" \
  "v_lshrrev_b32 v32, 2, v176\n" \
  "v_mul_u32_u24 v32, 0x380, v32\n" \
  "v_and_b32 v34, 3, v176\n" \
  "v_lshl_add_u32 v32, v34, 3, v32\n" \
  "v_add_u32 v200, s33, v32\n" \
  "s_waitcnt vmcnt(9)\n" \
  "v_add_u32 v36, 0x3779b1, v180\n" \
  "v_or_b32 v36, 0xff800000, v36\n" \
  "s_nop 0\n" \
  "v_readfirstlane_b32 s67, v36\n" \
  "v_mov_b32 v204, v36\n" \
  "v_mov_b32 v205, v36\n" \
  "v_mov_b32 v206, v36\n" \
  "v_mov_b32 v207, v36\n" \
  "s_lshr_b32 s32, s65, 2\n" \
  "s_lshl_b32 s32, s32, 11\n" \
  "s_and_b32 s58, s65, 3\n" \
  "s_lshl_b32 s58, s58, 8\n" \
  "s_add_u32 s32, s32, s58\n" \
  "s_lshr_b32 s58, %[w], 1\n" \
  "s_lshl_b32 s58, s58, 6\n" \
  "s_add_u32 s32, s32, s58\n" \
  "s_and_b32 s58, %[w], 1\n" \
  "s_lshl_b32 s58, s58, 4\n" \
  "s_add_u32 s32, s32, s58\n" \
  "s_add_u32 s32, s32, 0x950000\n" \
  "v_mov_b32 v229, v36\n" \
  "v_and_b32 v32, 40, v176\n" \
  "v_and_b32 v34, 16, v176\n" \
  "v_lshl_add_u32 v32, v34, 6, v32\n" \
  "s_waitcnt vmcnt(8)\n" \
  "v_dot2_f32_bf16 v224, v56, v212, 0\n" \
  "v_dot2_f32_bf16 v225, v56, v56, 0\n" \
  "v_dot2_f32_bf16 v226, v57, v212, 0\n" \
  "v_dot2_f32_bf16 v227, v57, v57, 0\n" \
  "v_dot2_f32_bf16 v224, v58, v212, v224\n" \
  "v_dot2_f32_bf16 v225, v58, v58, v225\n" \
  "v_dot2_f32_bf16 v226, v59, v212, v226\n" \
  "v_dot2_f32_bf16 v227, v59, v59, v227\n" \
  "s_waitcnt vmcnt(7)\n" \
  "v_dot2_f32_bf16 v224, v64, v212, v224\n" \
  "v_dot2_f32_bf16 v225, v64, v64, v225\n" \
  "v_dot2_f32_bf16 v226, v65, v212, v226\n" \
  "v_dot2_f32_bf16 v227, v65, v65, v227\n" \
  "v_dot2_f32_bf16 v224, v66, v212, v224\n" \
  "v_dot2_f32_bf16 v225, v66, v66, v225\n" \
  "v_dot2_f32_bf16 v226, v67, v212, v226\n" \
  "v_dot2_f32_bf16 v227, v67, v67, v227\n" \
  "s_waitcnt vmcnt(6)\n" \
  "v_dot2_f32_bf16 v224, v72, v212, v224\n" \
  "v_dot2_f32_bf16 v225, v72, v72, v225\n" \
  "v_dot2_f32_bf16 v226, v73, v212, v226\n" \
  "v_dot2_f32_bf16 v227, v73, v73, v227\n" \
  "v_dot2_f32_bf16 v224, v74, v212, v224\n" \
  "v_dot2_f32_bf16 v225, v74, v74, v225\n" \
  "v_dot2_f32_bf16 v226, v75, v212, v226\n" \
  "v_dot2_f32_bf16 v227, v75, v75, v227\n" \
  "s_waitcnt vmcnt(5)\n" \
  "v_dot2_f32_bf16 v224, v80, v212, v224\n" \
  "v_dot2_f32_bf16 v225, v80, v80, v225\n" \
  "v_dot2_f32_bf16 v226, v81, v212, v226\n" \
  "v_dot2_f32_bf16 v227, v81, v81, v227\n" \
  "v_dot2_f32_bf16 v224, v82, v212, v224\n" \
  "v_dot2_f32_bf16 v225, v82, v82, v225\n" \
  "v_dot2_f32_bf16 v226, v83, v212, v226\n" \
  "v_dot2_f32_bf16 v227, v83, v83, v227\n" \
  "s_waitcnt vmcnt(4)\n" \
  "v_dot2_f32_bf16 v224, v88, v212, v224\n" \
  "v_dot2_f32_bf16 v225, v88, v88, v225\n" \
  "v_dot2_f32_bf16 v226, v89, v212, v226\n" \
  "v_dot2_f32_bf16 v227, v89, v89, v227\n" \
  "v_dot2_f32_bf16 v224, v90, v212, v224\n" \
  "v_dot2_f32_bf16 v225, v90, v90, v225\n" \
  "v_dot2_f32_bf16 v226, v91, v212, v226\n" \
  "v_dot2_f32_bf16 v227, v91, v91, v227\n" \
  "s_waitcnt vmcnt(3)\n" \
  "v_dot2_f32_bf16 v224, v96, v212, v224\n" \
  "v_dot2_f32_bf16 v225, v96, v96, v225\n" \
  "v_dot2_f32_bf16 v226, v97, v212, v226\n" \
  "v_dot2_f32_bf16 v227, v97, v97, v227\n" \
  "v_dot2_f32_bf16 v224, v98, v212, v224\n" \
  "v_dot2_f32_bf16 v225, v98, v98, v225\n" \
  "v_dot2_f32_bf16 v226, v99, v212, v226\n" \
  "v_dot2_f32_bf16 v227, v99, v99, v227\n" \
  "s_waitcnt vmcnt(2)\n" \
  "v_dot2_f32_bf16 v224, v104, v212, v224\n" \
  "v_dot2_f32_bf16 v225, v104, v104, v225\n" \
  "v_dot2_f32_bf16 v226, v105, v212, v226\n" \
  "v_dot2_f32_bf16 v227, v105, v105, v227\n" \
  "v_dot2_f32_bf16 v224, v106, v212, v224\n" \
  "v_dot2_f32_bf16 v225, v106, v106, v225\n" \
  "v_dot2_f32_bf16 v226, v107, v212, v226\n" \
  "v_dot2_f32_bf16 v227, v107, v107, v227\n" \
  "s_nop 1\n" \
  "v_add_f32 v224, v224, v226\n" \
  "v_add_f32 v225, v225, v227\n" \
  "s_nop 0\n" \
  "v_add_f32_dpp v224, v224, v224 quad_perm:[1,0,3,2] row_mask:0xf bank_mask:0xf\n" \
  "v_add_f32_dpp v225, v225, v225 quad_perm:[1,0,3,2] row_mask:0xf bank_mask:0xf\n" \
  "s_nop 0\n" \
  "v_add_f32_dpp v224, v224, v224 quad_perm:[2,3,0,1] row_mask:0xf bank_mask:0xf\n" \
  "v_add_f32_dpp v225, v225, v225 quad_perm:[2,3,0,1] row_mask:0xf bank_mask:0xf\n" \
  "s_nop 0\n" \
  "v_add_f32_dpp v224, v224, v224 row_half_mirror row_mask:0xf bank_mask:0xf\n" \
  "v_add_f32_dpp v225, v225, v225 row_half_mirror row_mask:0xf bank_mask:0xf\n" \
  "v_mul_f32 v224, 0x3b124925, v224\n" \
  "v_mul_f32 v225, 0x3b124925, v225\n" \
  "v_fma_f32 v225, -v224, v224, v225\n" \
  "v_max_f32 v225, 0, v225\n" \
  "v_cvt_pk_bf16_f32 v225, 0, v225\n" \
  "v_add_f32 v225, 0x3727c5ac, v225\n" \
  "v_cvt_pk_bf16_f32 v225, 0, v225\n" \
  "v_sqrt_f32 v225, v225\n" \
  "s_nop 0\n" \
  "v_cvt_pk_bf16_f32 v228, v224, v225\n" \
  "s_mov_b32 s58, 0x1010101\n" \
  "s_mov_b32 s59, 0x1010101\n" \
  "s_mov_b64 exec, s[58:59]\n" \
  "buffer_store_dwordx2 v[228:229], v32, s[28:31], s32 offen sc1\n" \
  "s_mov_b64 exec, -1\n" \
  "buffer_store_dwordx4 v[204:207], v0, s[28:31], s50 offen sc1\n" \
  "s_lshr_b32 s32, s65, 1\n" \
  "s_mul_i32 s33, s32, 0x6200\n" \
  "s_lshl_b32 s34, %[w], 10\n" \
  "v_add_u32 v32, 0, v28\n" \
  "v_min_u32 v32, 0x61f0, v32\n" \
  "v_add_u32 v32, s33, v32\n" \
  "s_add_u32 m0, s34, 0x15000\n" \
  "s_nop 0\n" \
  "global_load_lds_dwordx4 v32, s[22:23]\n" \
  "v_add_u32 v32, 0x2000, v28\n" \
  "v_min_u32 v32, 0x61f0, v32\n" \
  "v_add_u32 v32, s33, v32\n" \
  "s_add_u32 m0, s34, 0x17000\n" \
  "s_nop 0\n" \
  "global_load_lds_dwordx4 v32, s[22:23]\n" \
  "v_add_u32 v32, 0x4000, v28\n" \
  "v_min_u32 v32, 0x61f0, v32\n" \
  "v_add_u32 v32, s33, v32\n" \
  "s_add_u32 m0, s34, 0x19000\n" \
  "s_nop 0\n" \
  "global_load_lds_dwordx4 v32, s[22:23]\n" \
  "v_add_u32 v32, 0x6000, v28\n" \
  "v_min_u32 v32, 0x61f0, v32\n" \
  "v_add_u32 v32, s33, v32\n" \
  "s_add_u32 m0, s34, 0x1b000\n" \
  "s_nop 0\n" \
  "global_load_lds_dwordx4 v32, s[22:23]\n" \
  "s_waitcnt vmcnt(4)\n" \
  "s_waitcnt lgkmcnt(0)\n" \
  "s_barrier\n" \
  "s_lshl_b32 s58, s66, 4\n" \
  "s_add_u32 s58, s58, 0x2000\n" \
  "s_cmp_eq_u32 %[w], 7\n" \
  "s_cselect_b64 exec, 1, 0\n" \
  "buffer_store_dword v36, off, s[28:31], s58 sc1\n" \
  "s_mov_b64 exec, -1\n" \
  "buffer_store_dwordx4 v[204:207], v26, s[28:31], s50 offen sc1\n" \
  "buffer_store_dwordx4 v[48:51], v0, s[28:31], s53 offen sc1\n" \
  "s_min_u32 s32, %[w], 6\n" \
  "s_lshl_b32 s32, s32, 9\n" \
  "v_and_b32 v33, 31, v176\n" \
  "v_lshl_add_u32 v33, v33, 4, s32\n" \
  "s_mov_b32 s13, 0x1000\n" \
  "s_mov_b64 s[32:33], s[26:27]\n" \
  "s_mov_b32 s34, 0x1c0000\n" \
  "s_mov_b32 s35, 0x20000\n" \
  "s_waitcnt vmcnt(3)\n" \
  "s_waitcnt lgkmcnt(0)\n" \
  "s_barrier\n" \
  "ds_read_b64_tr_b16 v[204:205], v200\n" \
  "ds_read_b64_tr_b16 v[206:207], v200 offset:14336\n" \
  "ds_read_b64_tr_b16 v[208:209], v200 offset:256\n" \
  "ds_read_b64_tr_b16 v[210:211], v200 offset:14592\n" \
  "s_waitcnt lgkmcnt(2)\n" \
  "v_cndmask_b32 v206, v206, 0, s[60:61]\n" \
  "v_cndmask_b32 v207, v207, 0, s[60:61]\n" \
  "s_waitcnt lgkmcnt(0)\n" \
  "v_cndmask_b32 v210, v210, 0, s[60:61]\n" \
  "v_cndmask_b32 v211, v211, 0, s[60:61]\n" \
  "buffer_store_dwordx4 v[204:207], v0, s[28:31], s15 offen sc1\n" \
  "s_add_u32 s15, s15, 0x20000\n" \
  "s_mov_b64 exec, s[68:69]\n" \
  "buffer_store_dwordx4 v[208:211], v0, s[28:31], s15 offen sc1\n" \
  "s_mov_b64 exec, -1\n" \
  "buffer_load_dword v37, v33, s[28:31], s13 offen sc1\n" \
  "C_f0s:\n" \
  "s_waitcnt vmcnt(0)\n" \
  "v_cmp_ne_u32 vcc, s67, v37\n" \
  "s_nop 0\n" \
  "s_cbranch_vccnz F_f0s\n" \
  "K_f0s:\n" \
  "s_waitcnt lgkmcnt(0)\n" \
  "s_barrier\n" \
  "s_mov_b64 exec, s[68:69]\n" \
  "buffer_load_dwordx4 v[92:95], v0, s[28:31], s19 offen sc1\n" \
  "s_mov_b64 exec, -1\n" \
  "s_mov_b64 exec, s[68:69]\n" \
  "buffer_load_dwordx4 v[100:103], v0, s[28:31], s19 offen offset:2048 sc1\n" \
  "s_mov_b64 exec, -1\n" \
  "s_add_u32 s19, s19, 0x1000\n" \
  "s_mov_b64 exec, s[68:69]\n" \
  "buffer_load_dwordx4 v[108:111], v0, s[28:31], s19 offen sc1\n" \
  "s_mov_b64 exec, -1\n" \
  "s_mov_b64 exec, s[68:69]\n" \
  "buffer_load_dwordx4 v[116:119], v0, s[28:31], s19 offen offset:2048 sc1\n" \
  "s_mov_b64 exec, -1\n" \
  "s_add_u32 s19, s19, 0x1000\n" \
  "s_mov_b64 exec, s[68:69]\n" \
  "buffer_load_dwordx4 v[124:127], v0, s[28:31], s19 offen sc1\n" \
  "s_mov_b64 exec, -1\n" \
  "s_mov_b64 exec, s[68:69]\n" \
  "buffer_load_dwordx4 v[132:135], v0, s[28:31], s19 offen offset:2048 sc1\n" \
  "s_mov_b64 exec, -1\n" \
  "s_add_u32 s19, s19, 0x1000\n" \
  "s_mov_b64 exec, s[68:69]\n" \
  "buffer_load_dwordx4 v[140:143], v0, s[28:31], s19 offen sc1\n" \
  "s_mov_b64 exec, -1\n" \
  "s_mov_b64 exec, s[68:69]\n" \
  "buffer_load_dwordx4 v[148:151], v0, s[28:31], s19 offen offset:2048 sc1\n" \
  "s_mov_b64 exec, -1\n" \
  "s_add_u32 s19, s19, 0x1000\n" \
  "s_mov_b64 exec, s[68:69]\n" \
  "buffer_load_dwordx4 v[156:159], v0, s[28:31], s19 offen sc1\n" \
  "s_mov_b64 exec, -1\n" \
  "s_mov_b64 exec, s[68:69]\n" \
  "buffer_load_dwordx4 v[164:167], v0, s[28:31], s19 offen offset:2048 sc1\n" \
  "s_mov_b64 exec, -1\n" \
  "s_add_u32 s19, s19, 0x1000\n" \
  "s_mov_b64 exec, s[68:69]\n" \
  "buffer_load_dwordx4 v[60:63], v0, s[28:31], s19 offen sc1\n" \
  "s_mov_b64 exec, -1\n" \
  "s_mov_b64 exec, s[68:69]\n" \
  "buffer_load_dwordx4 v[232:235], v0, s[28:31], s19 offen offset:2048 sc1\n" \
  "s_mov_b64 exec, -1\n" \
  "s_add_u32 s19, s19, 0x1000\n" \
  "s_mov_b64 exec, s[68:69]\n" \
  "buffer_load_dwordx4 v[236:239], v0, s[28:31], s19 offen sc1\n" \
  "s_mov_b64 exec, -1\n" \
  "s_mov_b64 exec, s[68:69]\n" \
  "buffer_load_dwordx4 v[136:139], v0, s[28:31], s19 offen offset:2048 sc1\n" \
  "s_mov_b64 exec, -1\n" \
  "s_cmp_gt_u32 %[w], 5\n" \
  "s_cbranch_scc1 WS1\n" \
  "v_add_u32 v52, s12, v0\n" \
  "v_mov_b32 v248, 0x3f803f80\n" \
  "v_mov_b32 v249, 0x3f803f80\n" \
  "v_mov_b32 v250, 0x3f803f80\n" \
  "v_mov_b32 v251, 0x3f803f80\n" \
  "s_lshl_b32 s58, %[w], 6\n" \
  "v_add_u32 v53, s58, v10\n" \
  "s_mov_b32 s58, 0x10001\n" \
  "s_mov_b32 s59, 0x10001\n" \
  "s_waitcnt vmcnt(13)\n" \
  "ds_write_b128 v52, v[92:95]\n" \
  "v_mfma_f32_16x16x32_bf16 v[44:47], v[92:95], v[248:251], 0\n" \
  "s_waitcnt vmcnt(12)\n" \
  "ds_write_b128 v52, v[100:103] offset:2048\n" \
  "v_mfma_f32_16x16x32_bf16 v[40:43], v[100:103], v[248:251], 0\n" \
  "s_waitcnt vmcnt(11)\n" \
  "ds_write_b128 v52, v[108:111] offset:4096\n" \
  "v_mfma_f32_16x16x32_bf16 v[44:47], v[108:111], v[248:251], v[44:47]\n" \
  "s_waitcnt vmcnt(10)\n" \
  "ds_write_b128 v52, v[116:119] offset:6144\n" \
  "v_mfma_f32_16x16x32_bf16 v[40:43], v[116:119], v[248:251], v[40:43]\n" \
  "s_waitcnt vmcnt(9)\n" \
  "ds_write_b128 v52, v[124:127] offset:8192\n" \
  "v_mfma_f32_16x16x32_bf16 v[44:47], v[124:127], v[248:251], v[44:47]\n" \
  "s_waitcnt vmcnt(8)\n" \
  "ds_write_b128 v52, v[132:135] offset:10240\n" \
  "v_mfma_f32_16x16x32_bf16 v[40:43], v[132:135], v[248:251], v[40:43]\n" \
  "s_waitcnt vmcnt(7)\n" \
  "ds_write_b128 v52, v[140:143] offset:12288\n" \
  "v_mfma_f32_16x16x32_bf16 v[44:47], v[140:143], v[248:251], v[44:47]\n" \
  "s_waitcnt vmcnt(6)\n" \
  "ds_write_b128 v52, v[148:151] offset:14336\n" \
  "v_mfma_f32_16x16x32_bf16 v[40:43], v[148:151], v[248:251], v[40:43]\n" \
  "s_waitcnt vmcnt(5)\n" \
  "ds_write_b128 v52, v[156:159] offset:16384\n" \
  "v_mfma_f32_16x16x32_bf16 v[44:47], v[156:159], v[248:251], v[44:47]\n" \
  "s_waitcnt vmcnt(4)\n" \
  "ds_write_b128 v52, v[164:167] offset:18432\n" \
  "v_mfma_f32_16x16x32_bf16 v[40:43], v[164:167], v[248:251], v[40:43]\n" \
  "s_waitcnt vmcnt(3)\n" \
  "ds_write_b128 v52, v[60:63] offset:20480\n" \
  "v_mfma_f32_16x16x32_bf16 v[44:47], v[60:63], v[248:251], v[44:47]\n" \
  "s_waitcnt vmcnt(2)\n" \
  "ds_write_b128 v52, v[232:235] offset:22528\n" \
  "v_mfma_f32_16x16x32_bf16 v[40:43], v[232:235], v[248:251], v[40:43]\n" \
  "s_waitcnt vmcnt(1)\n" \
  "ds_write_b128 v52, v[236:239] offset:24576\n" \
  "v_mfma_f32_16x16x32_bf16 v[44:47], v[236:239], v[248:251], v[44:47]\n" \
  "s_waitcnt vmcnt(0)\n" \
  "ds_write_b128 v52, v[136:139] offset:26624\n" \
  "v_mfma_f32_16x16x32_bf16 v[40:43], v[136:139], v[248:251], v[40:43]\n" \
  "s_nop 7\n" \
  "v_add_f32 v44, v44, v40\n" \
  "v_add_f32 v45, v45, v41\n" \
  "v_add_f32 v46, v46, v42\n" \
  "v_add_f32 v47, v47, v43\n" \
  "s_mov_b64 exec, s[58:59]\n" \
  "ds_write_b128 v53, v[44:47]\n" \
  "s_mov_b64 exec, -1\n" \
  "WS1:\n" \
  "s_mov_b32 s19, 0x2000\n" \
  "s_waitcnt vmcnt(13)\n" \
  "buffer_load_dword v92, v0, s[28:31], s19 offen sc1\n" \
  "buffer_load_dword v93, v0, s[28:31], s19 offen offset:1024 sc1\n" \
  "buffer_load_dword v94, v0, s[28:31], s19 offen offset:2048 sc1\n" \
  "buffer_load_dword v95, v0, s[28:31], s19 offen offset:3072 sc1\n" \
  "C_fys:\n" \
  "s_waitcnt vmcnt(1)\n" \
  "v_max3_u32 v32, v92, v93, v94\n" \
  "v_min3_u32 v34, v92, v93, v94\n" \
  "s_waitcnt vmcnt(0)\n" \
  "v_max3_u32 v32, v32, v95, s67\n" \
  "v_min3_u32 v34, v34, v95, s67\n" \
  "v_cmp_ne_u32 vcc, v32, v34\n" \
  "s_nop 0\n" \
  "s_cbranch_vccnz F_fys\n" \
  "K_fys:\n" \
  "s_cmp_lg_u32 %[w], 0\n" \
  "s_cbranch_scc1 SPW\n" \
  "SPL:\n" \
  "buffer_load_dword v37, off, s[28:31], 0 sc1\n" \
  "s_sub_u32 s57, s57, 1\n" \
  "s_cbranch_scc1 SPX\n" \
  "s_waitcnt vmcnt(0)\n" \
  "v_cmp_ne_u32 vcc, s67, v37\n" \
  "s_sleep 2\n" \
  "s_cbranch_vccnz SPL\n" \
  "SPX:\n" \
  "s_max_i32 s57, s57, 0\n" \
  "SPW:\n" \
  "s_waitcnt vmcnt(0)\n" \
  "s_nop 7\n" \
  "s_branch PREP\n" \
  "F_st2:\n" \
  "s_cmp_eq_u32 s57, 0\n" \
  "s_cbranch_scc1 K_st2\n" \
  "s_sub_u32 s57, s57, 1\n" \
  "s_waitcnt vmcnt(0)\n" \
  "s_sleep 2\n" \
  "v_cmp_eq_u32 vcc, -1, v157\n" \
  "s_nop 0\n" \
  "s_mov_b64 exec, vcc\n" \
  "buffer_load_dwordx2 v[156:157], v48, s[28:31], s40 offen sc1\n" \
  "s_mov_b64 exec, -1\n" \
  "s_waitcnt vmcnt(0)\n" \
  "s_nop 7\n" \
  "s_branch C_st2\n" \
  "F_x2_0:\n" \
  "s_cmp_eq_u32 s57, 0\n" \
  "s_cbranch_scc1 K_x2_0\n" \
  "s_sub_u32 s57, s57, 1\n" \
  "s_waitcnt vmcnt(0)\n" \
  "s_sleep 2\n" \
  "v_max_u32 v34, v57, v59\n" \
  "v_cmp_eq_u32 vcc, -1, v34\n" \
  "s_nop 0\n" \
  "s_mov_b64 exec, vcc\n" \
  "buffer_load_dwordx4 v[56:59], v2, s[28:31], s46 offen sc1\n" \
  "s_mov_b64 exec, -1\n" \
  "v_max_u32 v34, v65, v67\n" \
  "v_cmp_eq_u32 vcc, -1, v34\n" \
  "s_nop 0\n" \
  "s_mov_b64 exec, vcc\n" \
  "buffer_load_dwordx4 v[64:67], v2, s[28:31], s46 offen offset:1024 sc1\n" \
  "s_mov_b64 exec, -1\n" \
  "v_max_u32 v34, v73, v75\n" \
  "v_cmp_eq_u32 vcc, -1, v34\n" \
  "s_nop 0\n" \
  "s_mov_b64 exec, vcc\n" \
  "buffer_load_dwordx4 v[72:75], v2, s[28:31], s46 offen offset:2048 sc1\n" \
  "s_mov_b64 exec, -1\n" \
  "v_max_u32 v34, v81, v83\n" \
  "v_cmp_eq_u32 vcc, -1, v34\n" \
  "s_nop 0\n" \
  "s_mov_b64 exec, vcc\n" \
  "buffer_load_dwordx4 v[80:83], v2, s[28:31], s46 offen offset:3072 sc1\n" \
  "s_mov_b64 exec, -1\n" \
  "v_max_u32 v34, v89, v91\n" \
  "v_cmp_eq_u32 vcc, -1, v34\n" \
  "s_nop 0\n" \
  "s_mov_b64 exec, vcc\n" \
  "buffer_load_dwordx4 v[88:91], v2, s[28:31], s47 offen sc1\n" \
  "s_mov_b64 exec, -1\n" \
  "v_max_u32 v34, v97, v99\n" \
  "v_cmp_eq_u32 vcc, -1, v34\n" \
  "s_nop 0\n" \
  "s_mov_b64 exec, vcc\n" \
  "buffer_load_dwordx4 v[96:99], v2, s[28:31], s47 offen offset:1024 sc1\n" \
  "s_mov_b64 exec, -1\n" \
  "s_waitcnt vmcnt(0)\n" \
  "s_nop 7\n" \
  "s_branch C_x2_0\n" \
  "F_x2_2:\n" \
  "s_cmp_eq_u32 s57, 0\n" \
  "s_cbranch_scc1 K_x2_2\n" \
  "s_sub_u32 s57, s57, 1\n" \
  "s_waitcnt vmcnt(0)\n" \
  "s_sleep 2\n" \
  "v_max_u32 v34, v73, v75\n" \
  "v_cmp_eq_u32 vcc, -1, v34\n" \
  "s_nop 0\n" \
  "s_mov_b64 exec, vcc\n" \
  "buffer_load_dwordx4 v[72:75], v2, s[28:31], s46 offen offset:2048 sc1\n" \
  "s_mov_b64 exec, -1\n" \
  "v_max_u32 v34, v81, v83\n" \
  "v_cmp_eq_u32 vcc, -1, v34\n" \
  "s_nop 0\n" \
  "s_mov_b64 exec, vcc\n" \
  "buffer_load_dwordx4 v[80:83], v2, s[28:31], s46 offen offset:3072 sc1\n" \
  "s_mov_b64 exec, -1\n" \
  "v_max_u32 v34, v89, v91\n" \
  "v_cmp_eq_u32 vcc, -1, v34\n" \
  "s_nop 0\n" \
  "s_mov_b64 exec, vcc\n" \
  "buffer_load_dwordx4 v[88:91], v2, s[28:31], s47 offen sc1\n" \
  "s_mov_b64 exec, -1\n" \
  "v_max_u32 v34, v97, v99\n" \
  "v_cmp_eq_u32 vcc, -1, v34\n" \
  "s_nop 0\n" \
  "s_mov_b64 exec, vcc\n" \
  "buffer_load_dwordx4 v[96:99], v2, s[28:31], s47 offen offset:1024 sc1\n" \
  "s_mov_b64 exec, -1\n" \
  "s_waitcnt vmcnt(0)\n" \
  "v_max3_u32 v34, v73, v75, v81\n" \
  "v_max_u32 v34, v34, v83\n" \
  "v_cmp_eq_u32 vcc, -1, v34\n" \
  "s_nop 0\n" \
  "s_cbranch_vccnz F_x2_2\n" \
  "s_branch K_x2_2\n" \
  "F_x2_4:\n" \
  "s_cmp_eq_u32 s57, 0\n" \
  "s_cbranch_scc1 K_x2_4\n" \
  "s_sub_u32 s57, s57, 1\n" \
  "s_waitcnt vmcnt(0)\n" \
  "s_sleep 2\n" \
  "v_max_u32 v34, v89, v91\n" \
  "v_cmp_eq_u32 vcc, -1, v34\n" \
  "s_nop 0\n" \
  "s_mov_b64 exec, vcc\n" \
  "buffer_load_dwordx4 v[88:91], v2, s[28:31], s47 offen sc1\n" \
  "s_mov_b64 exec, -1\n" \
  "v_max_u32 v34, v97, v99\n" \
  "v_cmp_eq_u32 vcc, -1, v34\n" \
  "s_nop 0\n" \
  "s_mov_b64 exec, vcc\n" \
  "buffer_load_dwordx4 v[96:99], v2, s[28:31], s47 offen offset:1024 sc1\n" \
  "s_mov_b64 exec, -1\n" \
  "v_max_u32 v34, v105, v107\n" \
  "v_cmp_eq_u32 vcc, -1, v34\n" \
  "s_nop 0\n" \
  "s_mov_b64 exec, vcc\n" \
  "buffer_load_dwordx4 v[104:107], v2, s[28:31], s47 offen offset:2048 sc1\n" \
  "s_mov_b64 exec, -1\n" \
  "v_max_u32 v34, v113, v115\n" \
  "v_cmp_eq_u32 vcc, -1, v34\n" \
  "s_nop 0\n" \
  "s_mov_b64 exec, vcc\n" \
  "buffer_load_dwordx4 v[112:115], v2, s[28:31], s47 offen offset:3072 sc1\n" \
  "s_mov_b64 exec, -1\n" \
  "v_max_u32 v34, v121, v123\n" \
  "v_cmp_eq_u32 vcc, -1, v34\n" \
  "s_nop 0\n" \
  "s_mov_b64 exec, vcc\n" \
  "buffer_load_dwordx4 v[120:123], v2, s[28:31], s48 offen sc1\n" \
  "s_mov_b64 exec, -1\n" \
  "s_waitcnt vmcnt(0)\n" \
  "v_max3_u32 v34, v89, v91, v97\n" \
  "v_max_u32 v34, v34, v99\n" \
  "v_cmp_eq_u32 vcc, -1, v34\n" \
  "s_nop 0\n" \
  "s_cbranch_vccnz F_x2_4\n" \
  "s_branch K_x2_4\n" \
  "F_x2_6:\n" \
  "s_cmp_eq_u32 s57, 0\n" \
  "s_cbranch_scc1 K_x2_6\n" \
  "s_sub_u32 s57, s57, 1\n" \
  "s_waitcnt vmcnt(0)\n" \
  "s_sleep 2\n" \
  "v_max_u32 v34, v105, v107\n" \
  "v_cmp_eq_u32 vcc, -1, v34\n" \
  "s_nop 0\n" \
  "s_mov_b64 exec, vcc\n" \
  "buffer_load_dwordx4 v[104:107], v2, s[28:31], s47 offen offset:2048 sc1\n" \
  "s_mov_b64 exec, -1\n" \
  "v_max_u32 v34, v113, v115\n" \
  "v_cmp_eq_u32 vcc, -1, v34\n" \
  "s_nop 0\n" \
  "s_mov_b64 exec, vcc\n" \
  "buffer_load_dwordx4 v[112:115], v2, s[28:31], s47 offen offset:3072 sc1\n" \
  "s_mov_b64 exec, -1\n" \
  "v_max_u32 v34, v121, v123\n" \
  "v_cmp_eq_u32 vcc, -1, v34\n" \
  "s_nop 0\n" \
  "s_mov_b64 exec, vcc\n" \
  "buffer_load_dwordx4 v[120:123], v2, s[28:31], s48 offen sc1\n" \
  "s_mov_b64 exec, -1\n" \
  "v_max_u32 v34, v129, v131\n" \
  "v_cmp_eq_u32 vcc, -1, v34\n" \
  "s_nop 0\n" \
  "s_mov_b64 exec, vcc\n" \
  "buffer_load_dwordx4 v[128:131], v2, s[28:31], s48 offen offset:1024 sc1\n" \
  "s_mov_b64 exec, -1\n" \
  "v_max_u32 v34, v137, v139\n" \
  "v_cmp_eq_u32 vcc, -1, v34\n" \
  "s_nop 0\n" \
  "s_mov_b64 exec, vcc\n" \
  "buffer_load_dwordx4 v[136:139], v2, s[28:31], s48 offen offset:2048 sc1\n" \
  "s_mov_b64 exec, -1\n" \
  "s_waitcnt vmcnt(0)\n" \
  "v_max3_u32 v34, v105, v107, v113\n" \
  "v_max_u32 v34, v34, v115\n" \
  "v_cmp_eq_u32 vcc, -1, v34\n" \
  "s_nop 0\n" \
  "s_cbranch_vccnz F_x2_6\n" \
  "s_branch K_x2_6\n" \
  "F_x2_8:\n" \
  "s_cmp_eq_u32 s57, 0\n" \
  "s_cbranch_scc1 K_x2_8\n" \
  "s_sub_u32 s57, s57, 1\n" \
  "s_waitcnt vmcnt(0)\n" \
  "s_sleep 2\n" \
  "v_max_u32 v34, v121, v123\n" \
  "v_cmp_eq_u32 vcc, -1, v34\n" \
  "s_nop 0\n" \
  "s_mov_b64 exec, vcc\n" \
  "buffer_load_dwordx4 v[120:123], v2, s[28:31], s48 offen sc1\n" \
  "s_mov_b64 exec, -1\n" \
  "v_max_u32 v34, v129, v131\n" \
  "v_cmp_eq_u32 vcc, -1, v34\n" \
  "s_nop 0\n" \
  "s_mov_b64 exec, vcc\n" \
  "buffer_load_dwordx4 v[128:131], v2, s[28:31], s48 offen offset:1024 sc1\n" \
  "s_mov_b64 exec, -1\n" \
  "v_max_u32 v34, v137, v139\n" \
  "v_cmp_eq_u32 vcc, -1, v34\n" \
  "s_nop 0\n" \
  "s_mov_b64 exec, vcc\n" \
  "buffer_load_dwordx4 v[136:139], v2, s[28:31], s48 offen offset:2048 sc1\n" \
  "s_mov_b64 exec, -1\n" \
  "v_max_u32 v34, v145, v147\n" \
  "v_cmp_eq_u32 vcc, -1, v34\n" \
  "s_nop 0\n" \
  "s_mov_b64 exec, vcc\n" \
  "buffer_load_dwordx4 v[144:147], v2, s[28:31], s48 offen offset:3072 sc1\n" \
  "s_mov_b64 exec, -1\n" \
  "v_max_u32 v34, v153, v155\n" \
  "v_cmp_eq_u32 vcc, -1, v34\n" \
  "s_nop 0\n" \
  "s_mov_b64 exec, vcc\n" \
  "buffer_load_dwordx4 v[152:155], v2, s[28:31], s49 offen sc1\n" \
  "s_mov_b64 exec, -1\n" \
  "s_waitcnt vmcnt(0)\n" \
  "v_max3_u32 v34, v121, v123, v129\n" \
  "v_max_u32 v34, v34, v131\n" \
  "v_cmp_eq_u32 vcc, -1, v34\n" \
  "s_nop 0\n" \
  "s_cbranch_vccnz F_x2_8\n" \
  "s_branch K_x2_8\n" \
  "F_x2_10:\n" \
  "s_cmp_eq_u32 s57, 0\n" \
  "s_cbranch_scc1 K_x2_10\n" \
  "s_sub_u32 s57, s57, 1\n" \
  "s_waitcnt vmcnt(0)\n" \
  "s_sleep 2\n" \
  "v_max_u32 v34, v137, v139\n" \
  "v_cmp_eq_u32 vcc, -1, v34\n" \
  "s_nop 0\n" \
  "s_mov_b64 exec, vcc\n" \
  "buffer_load_dwordx4 v[136:139], v2, s[28:31], s48 offen offset:2048 sc1\n" \
  "s_mov_b64 exec, -1\n" \
  "v_max_u32 v34, v145, v147\n" \
  "v_cmp_eq_u32 vcc, -1, v34\n" \
  "s_nop 0\n" \
  "s_mov_b64 exec, vcc\n" \
  "buffer_load_dwordx4 v[144:147], v2, s[28:31], s48 offen offset:3072 sc1\n" \
  "s_mov_b64 exec, -1\n" \
  "v_max_u32 v34, v153, v155\n" \
  "v_cmp_eq_u32 vcc, -1, v34\n" \
  "s_nop 0\n" \
  "s_mov_b64 exec, vcc\n" \
  "buffer_load_dwordx4 v[152:155], v2, s[28:31], s49 offen sc1\n" \
  "s_mov_b64 exec, -1\n" \
  "v_max_u32 v34, v161, v163\n" \
  "v_cmp_eq_u32 vcc, -1, v34\n" \
  "s_nop 0\n" \
  "s_mov_b64 exec, vcc\n" \
  "buffer_load_dwordx4 v[160:163], v2, s[28:31], s49 offen offset:1024 sc1\n" \
  "s_mov_b64 exec, -1\n" \
  "s_waitcnt vmcnt(0)\n" \
  "v_max3_u32 v34, v137, v139, v145\n" \
  "v_max_u32 v34, v34, v147\n" \
  "v_cmp_eq_u32 vcc, -1, v34\n" \
  "s_nop 0\n" \
  "s_cbranch_vccnz F_x2_10\n" \
  "s_branch K_x2_10\n" \
  "F_x2_12:\n" \
  "s_cmp_eq_u32 s57, 0\n" \
  "s_cbranch_scc1 K_x2_12\n" \
  "s_sub_u32 s57, s57, 1\n" \
  "s_waitcnt vmcnt(0)\n" \
  "s_sleep 2\n" \
  "v_max_u32 v34, v153, v155\n" \
  "v_cmp_eq_u32 vcc, -1, v34\n" \
  "s_nop 0\n" \
  "s_mov_b64 exec, vcc\n" \
  "buffer_load_dwordx4 v[152:155], v2, s[28:31], s49 offen sc1\n" \
  "s_mov_b64 exec, -1\n" \
  "v_max_u32 v34, v161, v163\n" \
  "v_cmp_eq_u32 vcc, -1, v34\n" \
  "s_nop 0\n" \
  "s_mov_b64 exec, vcc\n" \
  "buffer_load_dwordx4 v[160:163], v2, s[28:31], s49 offen offset:1024 sc1\n" \
  "s_mov_b64 exec, -1\n" \
  "s_waitcnt vmcnt(0)\n" \
  "v_max3_u32 v34, v153, v155, v161\n" \
  "v_max_u32 v34, v34, v163\n" \
  "v_cmp_eq_u32 vcc, -1, v34\n" \
  "s_nop 0\n" \
  "s_cbranch_vccnz F_x2_12\n" \
  "s_branch K_x2_12\n" \
  "F_kv:\n" \
  "s_cmp_eq_u32 s57, 0\n" \
  "s_cbranch_scc1 K_kv\n" \
  "s_sub_u32 s57, s57, 1\n" \
  "s_sleep 2\n" \
  "s_mov_b64 exec, vcc\n" \
  "buffer_load_dwordx4 v[240:243], v28, s[28:31], s10 offen sc1\n" \
  "buffer_load_dwordx4 v[244:247], v28, s[28:31], s11 offen sc1\n" \
  "s_mov_b64 exec, -1\n" \
  "s_waitcnt vmcnt(0)\n" \
  "s_branch C_kv\n" \
  "F_f10:\n" \
  "s_cmp_eq_u32 s57, 0\n" \
  "s_cbranch_scc1 K_f10\n" \
  "s_sub_u32 s57, s57, 1\n" \
  "s_sleep 2\n" \
  "s_mov_b64 exec, vcc\n" \
  "buffer_load_dword v37, v33, s[28:31], s13 offen sc1\n" \
  "s_mov_b64 exec, -1\n" \
  "s_waitcnt vmcnt(0)\n" \
  "s_branch C_f10\n" \
  "F_xg0:\n" \
  "s_cmp_eq_u32 s57, 0\n" \
  "s_cbranch_scc1 K_xg0\n" \
  "s_sub_u32 s57, s57, 1\n" \
  "s_sleep 2\n" \
  "s_mov_b64 exec, vcc\n" \
  "buffer_load_dwordx4 v[76:79], v29, s[28:31], 0 offen sc1\n" \
  "buffer_load_dwordx4 v[84:87], v29, s[28:31], 0 offen offset:1024 sc1\n" \
  "s_mov_b64 exec, -1\n" \
  "s_waitcnt vmcnt(0)\n" \
  "s_branch C_xg0\n" \
  "F_f11:\n" \
  "s_cmp_eq_u32 s57, 0\n" \
  "s_cbranch_scc1 K_f11\n" \
  "s_sub_u32 s57, s57, 1\n" \
  "s_sleep 2\n" \
  "s_mov_b64 exec, vcc\n" \
  "buffer_load_dword v37, v33, s[28:31], s13 offen sc1\n" \
  "s_mov_b64 exec, -1\n" \
  "s_waitcnt vmcnt(0)\n" \
  "s_branch C_f11\n" \
  "F_xg1:\n" \
  "s_cmp_eq_u32 s57, 0\n" \
  "s_cbranch_scc1 K_xg1\n" \
  "s_sub_u32 s57, s57, 1\n" \
  "s_sleep 2\n" \
  "s_mov_b64 exec, vcc\n" \
  "buffer_load_dwordx4 v[76:79], v29, s[28:31], 0 offen sc1\n" \
  "buffer_load_dwordx4 v[84:87], v29, s[28:31], 0 offen offset:1024 sc1\n" \
  "s_mov_b64 exec, -1\n" \
  "s_waitcnt vmcnt(0)\n" \
  "s_branch C_xg1\n" \
  "F_fy:\n" \
  "s_cmp_eq_u32 s57, 0\n" \
  "s_cbranch_scc1 K_fy\n" \
  "s_sub_u32 s57, s57, 1\n" \
  "s_sleep 2\n" \
  "s_mov_b64 exec, vcc\n" \
  "buffer_load_dword v92, v0, s[28:31], s19 offen sc1\n" \
  "buffer_load_dword v93, v0, s[28:31], s19 offen offset:1024 sc1\n" \
  "buffer_load_dword v94, v0, s[28:31], s19 offen offset:2048 sc1\n" \
  "buffer_load_dword v95, v0, s[28:31], s19 offen offset:3072 sc1\n" \
  "s_mov_b64 exec, -1\n" \
  "s_waitcnt vmcnt(0)\n" \
  "s_branch C_fy\n" \
  "F_f0s:\n" \
  "s_cmp_eq_u32 s57, 0\n" \
  "s_cbranch_scc1 K_f0s\n" \
  "s_sub_u32 s57, s57, 1\n" \
  "s_sleep 8\n" \
  "s_mov_b64 exec, vcc\n" \
  "buffer_load_dword v37, v33, s[28:31], s13 offen sc1\n" \
  "s_mov_b64 exec, -1\n" \
  "s_waitcnt vmcnt(0)\n" \
  "s_branch C_f0s\n" \
  "F_fys:\n" \
  "s_cmp_eq_u32 s57, 0\n" \
  "s_cbranch_scc1 K_fys\n" \
  "s_sub_u32 s57, s57, 1\n" \
  "s_sleep 2\n" \
  "s_mov_b64 exec, vcc\n" \
  "buffer_load_dword v92, v0, s[28:31], s19 offen sc1\n" \
  "buffer_load_dword v93, v0, s[28:31], s19 offen offset:1024 sc1\n" \
  "buffer_load_dword v94, v0, s[28:31], s19 offen offset:2048 sc1\n" \
  "buffer_load_dword v95, v0, s[28:31], s19 offen offset:3072 sc1\n" \
  "s_mov_b64 exec, -1\n" \
  "s_waitcnt vmcnt(0)\n" \
  "s_branch C_fys\n" \
  "F_y_0:\n" \
  "s_cmp_eq_u32 s57, 0\n" \
  "s_cbranch_scc1 K_y_0\n" \
  "s_sub_u32 s57, s57, 1\n" \
  "s_waitcnt vmcnt(0)\n" \
  "s_sleep 2\n" \
  "v_max_u32 v34, v169, v171\n" \
  "v_cmp_eq_u32 vcc, -1, v34\n" \
  "s_nop 0\n" \
  "s_mov_b64 exec, vcc\n" \
  "buffer_load_dwordx4 v[168:171], v1, s[28:31], s46 offen sc1\n" \
  "s_mov_b64 exec, -1\n" \
  "v_max_u32 v34, v173, v175\n" \
  "v_cmp_eq_u32 vcc, -1, v34\n" \
  "s_nop 0\n" \
  "s_mov_b64 exec, vcc\n" \
  "buffer_load_dwordx4 v[172:175], v1, s[28:31], s46 offen offset:1024 sc1\n" \
  "s_mov_b64 exec, -1\n" \
  "v_max_u32 v34, v177, v179\n" \
  "v_cmp_eq_u32 vcc, -1, v34\n" \
  "s_nop 0\n" \
  "s_mov_b64 exec, vcc\n" \
  "buffer_load_dwordx4 v[176:179], v1, s[28:31], s46 offen offset:2048 sc1\n" \
  "s_mov_b64 exec, -1\n" \
  "v_max_u32 v34, v181, v183\n" \
  "v_cmp_eq_u32 vcc, -1, v34\n" \
  "s_nop 0\n" \
  "s_mov_b64 exec, vcc\n" \
  "buffer_load_dwordx4 v[180:183], v1, s[28:31], s46 offen offset:3072 sc1\n" \
  "s_mov_b64 exec, -1\n" \
  "v_max_u32 v34, v185, v187\n" \
  "v_cmp_eq_u32 vcc, -1, v34\n" \
  "s_nop 0\n" \
  "s_mov_b64 exec, vcc\n" \
  "buffer_load_dwordx4 v[184:187], v1, s[28:31], s47 offen sc1\n" \
  "s_mov_b64 exec, -1\n" \
  "v_max_u32 v34, v189, v191\n" \
  "v_cmp_eq_u32 vcc, -1, v34\n" \
  "s_nop 0\n" \
  "s_mov_b64 exec, vcc\n" \
  "buffer_load_dwordx4 v[188:191], v1, s[28:31], s47 offen offset:1024 sc1\n" \
  "s_mov_b64 exec, -1\n" \
  "v_max_u32 v34, v193, v195\n" \
  "v_cmp_eq_u32 vcc, -1, v34\n" \
  "s_nop 0\n" \
  "s_mov_b64 exec, vcc\n" \
  "buffer_load_dwordx4 v[192:195], v1, s[28:31], s47 offen offset:2048 sc1\n" \
  "s_mov_b64 exec, -1\n" \
  "v_max_u32 v34, v197, v199\n" \
  "v_cmp_eq_u32 vcc, -1, v34\n" \
  "s_nop 0\n" \
  "s_mov_b64 exec, vcc\n" \
  "buffer_load_dwordx4 v[196:199], v1, s[28:31], s47 offen offset:3072 sc1\n" \
  "s_mov_b64 exec, -1\n" \
  "v_max_u32 v34, v201, v203\n" \
  "v_cmp_eq_u32 vcc, -1, v34\n" \
  "s_nop 0\n" \
  "s_mov_b64 exec, vcc\n" \
  "buffer_load_dwordx4 v[200:203], v1, s[28:31], s48 offen sc1\n" \
  "s_mov_b64 exec, -1\n" \
  "v_max_u32 v34, v205, v207\n" \
  "v_cmp_eq_u32 vcc, -1, v34\n" \
  "s_nop 0\n" \
  "s_mov_b64 exec, vcc\n" \
  "buffer_load_dwordx4 v[204:207], v1, s[28:31], s48 offen offset:1024 sc1\n" \
  "s_mov_b64 exec, -1\n" \
  "s_waitcnt vmcnt(0)\n" \
  "s_nop 7\n" \
  "s_branch C_y_0\n" \
  "F_y_2:\n" \
  "s_cmp_eq_u32 s57, 0\n" \
  "s_cbranch_scc1 K_y_2\n" \
  "s_sub_u32 s57, s57, 1\n" \
  "s_waitcnt vmcnt(0)\n" \
  "s_sleep 2\n" \
  "v_max_u32 v34, v177, v179\n" \
  "v_cmp_eq_u32 vcc, -1, v34\n" \
  "s_nop 0\n" \
  "s_mov_b64 exec, vcc\n" \
  "buffer_load_dwordx4 v[176:179], v1, s[28:31], s46 offen offset:2048 sc1\n" \
  "s_mov_b64 exec, -1\n" \
  "v_max_u32 v34, v181, v183\n" \
  "v_cmp_eq_u32 vcc, -1, v34\n" \
  "s_nop 0\n" \
  "s_mov_b64 exec, vcc\n" \
  "buffer_load_dwordx4 v[180:183], v1, s[28:31], s46 offen offset:3072 sc1\n" \
  "s_mov_b64 exec, -1\n" \
  "v_max_u32 v34, v185, v187\n" \
  "v_cmp_eq_u32 vcc, -1, v34\n" \
  "s_nop 0\n" \
  "s_mov_b64 exec, vcc\n" \
  "buffer_load_dwordx4 v[184:187], v1, s[28:31], s47 offen sc1\n" \
  "s_mov_b64 exec, -1\n" \
  "v_max_u32 v34, v189, v191\n" \
  "v_cmp_eq_u32 vcc, -1, v34\n" \
  "s_nop 0\n" \
  "s_mov_b64 exec, vcc\n" \
  "buffer_load_dwordx4 v[188:191], v1, s[28:31], s47 offen offset:1024 sc1\n" \
  "s_mov_b64 exec, -1\n" \
  "v_max_u32 v34, v193, v195\n" \
  "v_cmp_eq_u32 vcc, -1, v34\n" \
  "s_nop 0\n" \
  "s_mov_b64 exec, vcc\n" \
  "buffer_load_dwordx4 v[192:195], v1, s[28:31], s47 offen offset:2048 sc1\n" \
  "s_mov_b64 exec, -1\n" \
  "v_max_u32 v34, v197, v199\n" \
  "v_cmp_eq_u32 vcc, -1, v34\n" \
  "s_nop 0\n" \
  "s_mov_b64 exec, vcc\n" \
  "buffer_load_dwordx4 v[196:199], v1, s[28:31], s47 offen offset:3072 sc1\n" \
  "s_mov_b64 exec, -1\n" \
  "s_waitcnt vmcnt(0)\n" \
  "v_max3_u32 v34, v177, v179, v181\n" \
  "v_max_u32 v34, v34, v183\n" \
  "v_cmp_eq_u32 vcc, -1, v34\n" \
  "s_nop 0\n" \
  "s_cbranch_vccnz F_y_2\n" \
  "s_branch K_y_2\n" \
  "F_y_4:\n" \
  "s_cmp_eq_u32 s57, 0\n" \
  "s_cbranch_scc1 K_y_4\n" \
  "s_sub_u32 s57, s57, 1\n" \
  "s_waitcnt vmcnt(0)\n" \
  "s_sleep 2\n" \
  "v_max_u32 v34, v185, v187\n" \
  "v_cmp_eq_u32 vcc, -1, v34\n" \
  "s_nop 0\n" \
  "s_mov_b64 exec, vcc\n" \
  "buffer_load_dwordx4 v[184:187], v1, s[28:31], s47 offen sc1\n" \
  "s_mov_b64 exec, -1\n" \
  "v_max_u32 v34, v189, v191\n" \
  "v_cmp_eq_u32 vcc, -1, v34\n" \
  "s_nop 0\n" \
  "s_mov_b64 exec, vcc\n" \
  "buffer_load_dwordx4 v[188:191], v1, s[28:31], s47 offen offset:1024 sc1\n" \
  "s_mov_b64 exec, -1\n" \
  "v_max_u32 v34, v193, v195\n" \
  "v_cmp_eq_u32 vcc, -1, v34\n" \
  "s_nop 0\n" \
  "s_mov_b64 exec, vcc\n" \
  "buffer_load_dwordx4 v[192:195], v1, s[28:31], s47 offen offset:2048 sc1\n" \
  "s_mov_b64 exec, -1\n" \
  "v_max_u32 v34, v197, v199\n" \
  "v_cmp_eq_u32 vcc, -1, v34\n" \
  "s_nop 0\n" \
  "s_mov_b64 exec, vcc\n" \
  "buffer_load_dwordx4 v[196:199], v1, s[28:31], s47 offen offset:3072 sc1\n" \
  "s_mov_b64 exec, -1\n" \
  "v_max_u32 v34, v201, v203\n" \
  "v_cmp_eq_u32 vcc, -1, v34\n" \
  "s_nop 0\n" \
  "s_mov_b64 exec, vcc\n" \
  "buffer_load_dwordx4 v[200:203], v1, s[28:31], s48 offen sc1\n" \
  "s_mov_b64 exec, -1\n" \
  "v_max_u32 v34, v205, v207\n" \
  "v_cmp_eq_u32 vcc, -1, v34\n" \
  "s_nop 0\n" \
  "s_mov_b64 exec, vcc\n" \
  "buffer_load_dwordx4 v[204:207], v1, s[28:31], s48 offen offset:1024 sc1\n" \
  "s_mov_b64 exec, -1\n" \
  "s_waitcnt vmcnt(0)\n" \
  "v_max3_u32 v34, v185, v187, v189\n" \
  "v_max_u32 v34, v34, v191\n" \
  "v_cmp_eq_u32 vcc, -1, v34\n" \
  "s_nop 0\n" \
  "s_cbranch_vccnz F_y_4\n" \
  "s_branch K_y_4\n" \
  "F_y_6:\n" \
  "s_cmp_eq_u32 s57, 0\n" \
  "s_cbranch_scc1 K_y_6\n" \
  "s_sub_u32 s57, s57, 1\n" \
  "s_waitcnt vmcnt(0)\n" \
  "s_sleep 2\n" \
  "v_max_u32 v34, v193, v195\n" \
  "v_cmp_eq_u32 vcc, -1, v34\n" \
  "s_nop 0\n" \
  "s_mov_b64 exec, vcc\n" \
  "buffer_load_dwordx4 v[192:195], v1, s[28:31], s47 offen offset:2048 sc1\n" \
  "s_mov_b64 exec, -1\n" \
  "v_max_u32 v34, v197, v199\n" \
  "v_cmp_eq_u32 vcc, -1, v34\n" \
  "s_nop 0\n" \
  "s_mov_b64 exec, vcc\n" \
  "buffer_load_dwordx4 v[196:199], v1, s[28:31], s47 offen offset:3072 sc1\n" \
  "s_mov_b64 exec, -1\n" \
  "v_max_u32 v34, v201, v203\n" \
  "v_cmp_eq_u32 vcc, -1, v34\n" \
  "s_nop 0\n" \
  "s_mov_b64 exec, vcc\n" \
  "buffer_load_dwordx4 v[200:203], v1, s[28:31], s48 offen sc1\n" \
  "s_mov_b64 exec, -1\n" \
  "v_max_u32 v34, v205, v207\n" \
  "v_cmp_eq_u32 vcc, -1, v34\n" \
  "s_nop 0\n" \
  "s_mov_b64 exec, vcc\n" \
  "buffer_load_dwordx4 v[204:207], v1, s[28:31], s48 offen offset:1024 sc1\n" \
  "s_mov_b64 exec, -1\n" \
  "v_max_u32 v34, v209, v211\n" \
  "v_cmp_eq_u32 vcc, -1, v34\n" \
  "s_nop 0\n" \
  "s_mov_b64 exec, vcc\n" \
  "buffer_load_dwordx4 v[208:211], v1, s[28:31], s48 offen offset:2048 sc1\n" \
  "s_mov_b64 exec, -1\n" \
  "v_max_u32 v34, v213, v215\n" \
  "v_cmp_eq_u32 vcc, -1, v34\n" \
  "s_nop 0\n" \
  "s_mov_b64 exec, vcc\n" \
  "buffer_load_dwordx4 v[212:215], v1, s[28:31], s48 offen offset:3072 sc1\n" \
  "s_mov_b64 exec, -1\n" \
  "s_waitcnt vmcnt(0)\n" \
  "v_max3_u32 v34, v193, v195, v197\n" \
  "v_max_u32 v34, v34, v199\n" \
  "v_cmp_eq_u32 vcc, -1, v34\n" \
  "s_nop 0\n" \
  "s_cbranch_vccnz F_y_6\n" \
  "s_branch K_y_6\n" \
  "F_y_8:\n" \
  "s_cmp_eq_u32 s57, 0\n" \
  "s_cbranch_scc1 K_y_8\n" \
  "s_sub_u32 s57, s57, 1\n" \
  "s_waitcnt vmcnt(0)\n" \
  "s_sleep 2\n" \
  "v_max_u32 v34, v201, v203\n" \
  "v_cmp_eq_u32 vcc, -1, v34\n" \
  "s_nop 0\n" \
  "s_mov_b64 exec, vcc\n" \
  "buffer_load_dwordx4 v[200:203], v1, s[28:31], s48 offen sc1\n" \
  "s_mov_b64 exec, -1\n" \
  "v_max_u32 v34, v205, v207\n" \
  "v_cmp_eq_u32 vcc, -1, v34\n" \
  "s_nop 0\n" \
  "s_mov_b64 exec, vcc\n" \
  "buffer_load_dwordx4 v[204:207], v1, s[28:31], s48 offen offset:1024 sc1\n" \
  "s_mov_b64 exec, -1\n" \
  "v_max_u32 v34, v209, v211\n" \
  "v_cmp_eq_u32 vcc, -1, v34\n" \
  "s_nop 0\n" \
  "s_mov_b64 exec, vcc\n" \
  "buffer_load_dwordx4 v[208:211], v1, s[28:31], s48 offen offset:2048 sc1\n" \
  "s_mov_b64 exec, -1\n" \
  "v_max_u32 v34, v213, v215\n" \
  "v_cmp_eq_u32 vcc, -1, v34\n" \
  "s_nop 0\n" \
  "s_mov_b64 exec, vcc\n" \
  "buffer_load_dwordx4 v[212:215], v1, s[28:31], s48 offen offset:3072 sc1\n" \
  "s_mov_b64 exec, -1\n" \
  "v_max_u32 v34, v217, v219\n" \
  "v_cmp_eq_u32 vcc, -1, v34\n" \
  "s_nop 0\n" \
  "s_mov_b64 exec, vcc\n" \
  "buffer_load_dwordx4 v[216:219], v1, s[28:31], s49 offen sc1\n" \
  "s_mov_b64 exec, -1\n" \
  "v_max_u32 v34, v221, v223\n" \
  "v_cmp_eq_u32 vcc, -1, v34\n" \
  "s_nop 0\n" \
  "s_mov_b64 exec, vcc\n" \
  "buffer_load_dwordx4 v[220:223], v1, s[28:31], s49 offen offset:1024 sc1\n" \
  "s_mov_b64 exec, -1\n" \
  "s_waitcnt vmcnt(0)\n" \
  "v_max3_u32 v34, v201, v203, v205\n" \
  "v_max_u32 v34, v34, v207\n" \
  "v_cmp_eq_u32 vcc, -1, v34\n" \
  "s_nop 0\n" \
  "s_cbranch_vccnz F_y_8\n" \
  "s_branch K_y_8\n" \
  "F_y_10:\n" \
  "s_cmp_eq_u32 s57, 0\n" \
  "s_cbranch_scc1 K_y_10\n" \
  "s_sub_u32 s57, s57, 1\n" \
  "s_waitcnt vmcnt(0)\n" \
  "s_sleep 2\n" \
  "v_max_u32 v34, v209, v211\n" \
  "v_cmp_eq_u32 vcc, -1, v34\n" \
  "s_nop 0\n" \
  "s_mov_b64 exec, vcc\n" \
  "buffer_load_dwordx4 v[208:211], v1, s[28:31], s48 offen offset:2048 sc1\n" \
  "s_mov_b64 exec, -1\n" \
  "v_max_u32 v34, v213, v215\n" \
  "v_cmp_eq_u32 vcc, -1, v34\n" \
  "s_nop 0\n" \
  "s_mov_b64 exec, vcc\n" \
  "buffer_load_dwordx4 v[212:215], v1, s[28:31], s48 offen offset:3072 sc1\n" \
  "s_mov_b64 exec, -1\n" \
  "v_max_u32 v34, v217, v219\n" \
  "v_cmp_eq_u32 vcc, -1, v34\n" \
  "s_nop 0\n" \
  "s_mov_b64 exec, vcc\n" \
  "buffer_load_dwordx4 v[216:219], v1, s[28:31], s49 offen sc1\n" \
  "s_mov_b64 exec, -1\n" \
  "v_max_u32 v34, v221, v223\n" \
  "v_cmp_eq_u32 vcc, -1, v34\n" \
  "s_nop 0\n" \
  "s_mov_b64 exec, vcc\n" \
  "buffer_load_dwordx4 v[220:223], v1, s[28:31], s49 offen offset:1024 sc1\n" \
  "s_mov_b64 exec, -1\n" \
  "s_waitcnt vmcnt(0)\n" \
  "v_max3_u32 v34, v209, v211, v213\n" \
  "v_max_u32 v34, v34, v215\n" \
  "v_cmp_eq_u32 vcc, -1, v34\n" \
  "s_nop 0\n" \
  "s_cbranch_vccnz F_y_10\n" \
  "s_branch K_y_10\n" \
  "F_y_12:\n" \
  "s_cmp_eq_u32 s57, 0\n" \
  "s_cbranch_scc1 K_y_12\n" \
  "s_sub_u32 s57, s57, 1\n" \
  "s_waitcnt vmcnt(0)\n" \
  "s_sleep 2\n" \
  "v_max_u32 v34, v217, v219\n" \
  "v_cmp_eq_u32 vcc, -1, v34\n" \
  "s_nop 0\n" \
  "s_mov_b64 exec, vcc\n" \
  "buffer_load_dwordx4 v[216:219], v1, s[28:31], s49 offen sc1\n" \
  "s_mov_b64 exec, -1\n" \
  "v_max_u32 v34, v221, v223\n" \
  "v_cmp_eq_u32 vcc, -1, v34\n" \
  "s_nop 0\n" \
  "s_mov_b64 exec, vcc\n" \
  "buffer_load_dwordx4 v[220:223], v1, s[28:31], s49 offen offset:1024 sc1\n" \
  "s_mov_b64 exec, -1\n" \
  "s_waitcnt vmcnt(0)\n" \
  "v_max3_u32 v34, v217, v219, v221\n" \
  "v_max_u32 v34, v34, v223\n" \
  "v_cmp_eq_u32 vcc, -1, v34\n" \
  "s_nop 0\n" \
  "s_cbranch_vccnz F_y_12\n" \
  "s_branch K_y_12\n" \
  "F_nk:\n" \
  "s_cmp_eq_u32 s57, 0\n" \
  "s_cbranch_scc1 K_nk\n" \
  "s_sub_u32 s57, s57, 1\n" \
  "s_waitcnt vmcnt(0)\n" \
  "v_max_u32 v34, v109, v111\n" \
  "v_cmp_eq_u32 vcc, s67, v34\n" \
  "s_nop 0\n" \
  "s_mov_b64 exec, vcc\n" \
  "buffer_load_dwordx4 v[108:111], v0, s[28:31], s51 offen sc1\n" \
  "s_mov_b64 exec, -1\n" \
  "v_max_u32 v34, v117, v119\n" \
  "v_cmp_eq_u32 vcc, s67, v34\n" \
  "s_nop 0\n" \
  "s_mov_b64 exec, vcc\n" \
  "buffer_load_dwordx4 v[116:119], v0, s[28:31], s52 offen sc1\n" \
  "s_mov_b64 exec, -1\n" \
  "s_waitcnt vmcnt(0)\n" \
  "s_nop 7\n" \
  "s_branch C_nk\n" \
  "F_f2:\n" \
  "s_cmp_eq_u32 s57, 0\n" \
  "s_cbranch_scc1 K_f2\n" \
  "s_sub_u32 s57, s57, 1\n" \
  "s_sleep 2\n" \
  "buffer_load_dword v140, v0, s[28:31], s64 offen sc1\n" \
  "s_waitcnt vmcnt(0)\n" \
  "s_nop 7\n" \
  "s_branch C_f2\n" \
  "F_nv:\n" \
  "s_cmp_eq_u32 s57, 0\n" \
  "s_cbranch_scc1 K_nv\n" \
  "s_sub_u32 s57, s57, 1\n" \
  "s_waitcnt vmcnt(0)\n" \
  "v_max_u32 v34, v141, v143\n" \
  "v_cmp_eq_u32 vcc, s67, v34\n" \
  "s_nop 0\n" \
  "s_mov_b64 exec, vcc\n" \
  "buffer_load_dwordx4 v[140:143], v26, s[28:31], s51 offen sc1\n" \
  "s_mov_b64 exec, -1\n" \
  "v_max_u32 v34, v145, v147\n" \
  "v_cmp_eq_u32 vcc, s67, v34\n" \
  "s_nop 0\n" \
  "s_mov_b64 exec, vcc\n" \
  "buffer_load_dwordx4 v[144:147], v26, s[28:31], s52 offen sc1\n" \
  "s_mov_b64 exec, -1\n" \
  "s_waitcnt vmcnt(0)\n" \
  "s_nop 7\n" \
  "s_branch C_nv\n" \
  "HOTEND:\n" \
  "F_o:\n" \
  "s_cmp_eq_u32 s57, 0\n" \
  "s_cbranch_scc1 K_o\n" \
  "s_sub_u32 s57, s57, 1\n" \
  "s_waitcnt vmcnt(0)\n" \
  "s_sleep 2\n" \
  "v_max_u32 v34, v137, v139\n" \
  "v_cmp_eq_u32 vcc, -1, v34\n" \
  "s_nop 0\n" \
  "s_mov_b64 exec, vcc\n" \
  "buffer_load_dwordx4 v[136:139], v0, s[28:31], s54 offen sc1\n" \
  "s_mov_b64 exec, -1\n" \
  "v_max_u32 v34, v141, v143\n" \
  "v_cmp_eq_u32 vcc, -1, v34\n" \
  "s_nop 0\n" \
  "s_mov_b64 exec, vcc\n" \
  "buffer_load_dwordx4 v[140:143], v0, s[28:31], s54 offen offset:1024 sc1\n" \
  "s_mov_b64 exec, -1\n" \
  "v_max_u32 v34, v145, v147\n" \
  "v_cmp_eq_u32 vcc, -1, v34\n" \
  "s_nop 0\n" \
  "s_mov_b64 exec, vcc\n" \
  "buffer_load_dwordx4 v[144:147], v0, s[28:31], s54 offen offset:2048 sc1\n" \
  "s_mov_b64 exec, -1\n" \
  "v_max_u32 v34, v149, v151\n" \
  "v_cmp_eq_u32 vcc, -1, v34\n" \
  "s_nop 0\n" \
  "s_mov_b64 exec, vcc\n" \
  "buffer_load_dwordx4 v[148:151], v0, s[28:31], s54 offen offset:3072 sc1\n" \
  "s_mov_b64 exec, -1\n" \
  "s_waitcnt vmcnt(0)\n" \
  "s_nop 7\n" \
  "s_branch C_o\n" \
  ""
#define LAYER_CLOBBER "memory", "scc", "vcc", "v0", "v1", "v2", "v3", "v4", "v5", "v6", "v7", "v8", "v9", "v10", "v11", "v12", "v13", "v14", "v15", "v16", "v17", "v18", "v19", "v20", "v21", "v22", "v23", "v24", "v25", "v26", "v27", "v28", "v29", "v30", "v31", "v32", "v33", "v34", "v35", "v36", "v37", "v38", "v39", "v40", "v41", "v42", "v43", "v44", "v45", "v46", "v47", "v48", "v49", "v50", "v51", "v52", "v53", "v54", "v55", "v56", "v57", "v58", "v59", "v60", "v61", "v62", "v63", "v64", "v65", "v66", "v67", "v68", "v69", "v70", "v71", "v72", "v73", "v74", "v75", "v76", "v77", "v78", "v79", "v80", "v81", "v82", "v83", "v84", "v85", "v86", "v87", "v88", "v89", "v90", "v91", "v92", "v93", "v94", "v95", "v96", "v97", "v98", "v99", "v100", "v101", "v102", "v103", "v104", "v105", "v106", "v107", "v108", "v109", "v110", "v111", "v112", "v113", "v114", "v115", "v116", "v117", "v118", "v119", "v120", "v121", "v122", "v123", "v124", "v125", "v126", "v127", "v128", "v129", "v130", "v131", "v132", "v133", "v134", "v135", "v136", "v137", "v138", "v139", "v140", "v141", "v142", "v143", "v144", "v145", "v146", "v147", "v148", "v149", "v150", "v151", "v152", "v153", "v154", "v155", "v156", "v157", "v158", "v159", "v160", "v161", "v162", "v163", "v164", "v165", "v166", "v167", "v168", "v169", "v170", "v171", "v172", "v173", "v174", "v175", "v176", "v177", "v178", "v179", "v180", "v181", "v182", "v183", "v184", "v185", "v186", "v187", "v188", "v189", "v190", "v191", "v192", "v193", "v194", "v195", "v196", "v197", "v198", "v199", "v200", "v201", "v202", "v203", "v204", "v205", "v206", "v207", "v208", "v209", "v210", "v211", "v212", "v213", "v214", "v215", "v216", "v217", "v218", "v219", "v220", "v221", "v222", "v223", "v224", "v225", "v226", "v227", "v228", "v229", "v230", "v231", "v232", "v233", "v234", "v235", "v236", "v237", "v238", "v239", "v240", "v241", "v242", "v243", "v244", "v245", "v246", "v247", "v248", "v249", "v250", "v251", "v252", "v253", "v254", "v255", "s8", "s9", "s10", "s11", "s12", "s13", "s14", "s15", "s16", "s17", "s18", "s19", "s20", "s21", "s22", "s23", "s24", "s25", "s26", "s27", "s28", "s29", "s30", "s31", "s33", "s34", "s35", "s36", "s37", "s38", "s39", "s40", "s41", "s42", "s43", "s44", "s45", "s46", "s47", "s48", "s49", "s50", "s51", "s52", "s53", "s54", "s55", "s56", "s57", "s58", "s59", "s60", "s61", "s62", "s63", "s64", "s65", "s66", "s67", "s68", "s69", "s70", "s71"

extern "C" __global__ void __launch_bounds__(512, 2)
k_all(const void* x0, const void* kin, const void* vin, const void* shk, const void* ssmk, const void* ssmD, const void* wqkv,
      const void* wo, void* ws, void* out) {
  const int w = __builtin_amdgcn_readfirstlane(threadIdx.x >> 6);
  asm volatile(LAYER_ASM : : [ka] "s"(__builtin_amdgcn_kernarg_segment_ptr()), [b] "s"(blockIdx.x), [w] "s"(w) : LAYER_CLOBBER);
}

extern "C" void kernel_launch(void* const* d_in, const int* in_sizes, int n_in, void* d_out, int out_size, void* d_ws,
                              size_t ws_size, hipStream_t stream) {
  (void)hipFuncSetAttribute((const void*)k_all, hipFuncAttributeMaxDynamicSharedMemorySize, 160 * 1024);
  k_all<<<256, 512, 160 * 1024, stream>>>(d_in[0], d_in[1], d_in[2], d_in[3], d_in[4], d_in[5], d_in[7], d_in[8], d_ws, d_out);
}
```
